```python
import torch

def _softmax(x, axis=-1):
    return torch.softmax(x, dim=axis)
def _gelu(x):
    return torch.nn.functional.gelu(x, approximate="tanh")
def _silu(x):
    return torch.nn.functional.silu(x)
def _sigmoid(x):
    return torch.sigmoid(x)
def _layernorm(x, axis=-1, eps=1e-5):
    m = x.mean(dim=axis, keepdim=True)
    v = x.var(dim=axis, keepdim=True, unbiased=False)
    return (x - m) / torch.sqrt(v + eps)
def _scatter_add(tgt, idx, src, axis=0):
    return tgt.clone().index_add_(axis, idx, src)
def _segment_sum(src, seg, n):
    out = torch.zeros((n,) + src.shape[1:], dtype=src.dtype)
    return out.index_add_(0, seg, src)
def _pad(x, pw):
    flat = tuple(v for pair in reversed(pw) for v in pair)
    return torch.nn.functional.pad(x, flat)

kernel_name = 'sequential_preLN__spectral_gfnet__glu_gated_geglu__prompt'

def setup_inputs():

    _w = (lambda a: (lambda t: t.to(torch.bfloat16) if t.is_floating_point() else t)(torch.as_tensor(a)))
    def _A_setup():
        B, S, D = 1, 2048, 1024
        Df = D // 2 + 1
        return {
            "x": _w((lambda *s: torch.randn(*s, dtype=torch.bfloat16) * 0.05)(B, S, D)),
            "w_r": _w((lambda *s: torch.randn(*s, dtype=torch.bfloat16) * 0.4)(S, Df) * 0.02),
            "w_i": _w((lambda *s: torch.randn(*s, dtype=torch.bfloat16) * 0.4)(S, Df) * 0.02),
        }
    _A_in = _A_setup()

    def _B_setup():
        B, S, Dm, Df = 1, 2048, 1024, 3584
        return {
            "x": _w((lambda *s: torch.randn(*s, dtype=torch.bfloat16) * 0.05)(B, S, Dm)),
            "w_gate": _w((lambda *s: torch.randn(*s, dtype=torch.bfloat16) * 0.4)(Dm, Df) * (Dm ** -0.5)),
            "w_up": _w((lambda *s: torch.randn(*s, dtype=torch.bfloat16) * 0.4)(Dm, Df) * (Dm ** -0.5)),
            "w_down": _w((lambda *s: torch.randn(*s, dtype=torch.bfloat16) * 0.4)(Df, Dm) * (Df ** -0.5)),
        }
    _B_in = _B_setup()

    return {
        "A_x": _A_in["x"],
        "A_w_r": _A_in["w_r"],
        "A_w_i": _A_in["w_i"],
        "B_x": _B_in["x"],
        "B_w_gate": _B_in["w_gate"],
        "B_w_up": _B_in["w_up"],
        "B_w_down": _B_in["w_down"],
    }

def reference(A_x, A_w_r, A_w_i, B_x, B_w_gate, B_w_up, B_w_down):
    def _A_ref(x, w_r, w_i):
        B, S, D = x.shape
        X = (lambda x, axes=(-2,-1): torch.fft.rfft2(x.float(), dim=axes))(x, axes=(-2, -1))
        W = w_r + 1j * w_i
        X = X * W
        y = (lambda x, s, axes=(-2,-1): torch.fft.irfft2(x, s=s, dim=axes).to(torch.bfloat16))(X, (S, D), axes=(-2, -1))
        return y

    def _B_ref(x, w_gate, w_up, w_down):
        g = torch.einsum("bsd,df->bsf", x, w_gate)
        u = torch.einsum("bsd,df->bsf", x, w_up)
        h = _gelu(g) * u
        return torch.einsum("bsf,fd->bsd", h, w_down)

    x = A_x
    h = x + _A_ref(_layernorm(x, axis=-1), A_w_r, A_w_i)
    h2 = h + _B_ref(_layernorm(h, axis=-1), B_w_gate, B_w_up, B_w_down)
    return h2
```

```cpp
#include <hip/hip_runtime.h>
#include <hip/hip_bf16.h>

typedef unsigned short u16;
typedef unsigned int u32;
typedef float f2 __attribute__((ext_vector_type(2)));
typedef float f32x4 __attribute__((ext_vector_type(4)));
typedef short bf16x8 __attribute__((ext_vector_type(8)));
typedef short s16x4 __attribute__((ext_vector_type(4)));
typedef unsigned u32x4 __attribute__((ext_vector_type(4)));
typedef unsigned u32x2 __attribute__((ext_vector_type(2)));
#define LAS __attribute__((address_space(3)))
#define GAS __attribute__((address_space(1)))

constexpr int S_ = 2048, D_ = 1024, DF = 513, FF = 3584;

__device__ __forceinline__ float bf2f(u32 h) { return __uint_as_float(h << 16); }
__device__ __forceinline__ u32 f2bf(float f) { __bf16 b = (__bf16)f; return (u32)__builtin_bit_cast(u16, b); }
__device__ __forceinline__ float bfr(float f) { return bf2f(f2bf(f)); }
__device__ __forceinline__ u32 pack2(float lo, float hi) { return f2bf(lo) | (f2bf(hi) << 16); }

__device__ __forceinline__ f2 cmul(f2 a, f2 b) { return f2{a.x * b.x - a.y * b.y, a.x * b.y + a.y * b.x}; }
__device__ __forceinline__ f2 cmulc(f2 a, f2 b) { return f2{a.x * b.x + a.y * b.y, a.y * b.x - a.x * b.y}; }
__device__ __forceinline__ f2 cconj(f2 a) { return f2{a.x, -a.y}; }
template <int D> __device__ __forceinline__ f2 muli(f2 a) { return D > 0 ? f2{-a.y, a.x} : f2{a.y, -a.x}; }
template <int D> __device__ __forceinline__ f2 twm(f2 a, f2 w) { return D > 0 ? cmulc(a, w) : cmul(a, w); }

template <int D> __device__ __forceinline__ void dft4(f2& x0, f2& x1, f2& x2, f2& x3) {
    f2 t0 = x0 + x2, t1 = x0 - x2, t2 = x1 + x3, t3 = muli<D>(x1 - x3);
    x0 = t0 + t2; x2 = t0 - t2; x1 = t1 + t3; x3 = t1 - t3;
}
template <int D> __device__ __forceinline__ void dft8(f2* x) {
    dft4<D>(x[0], x[2], x[4], x[6]);
    dft4<D>(x[1], x[3], x[5], x[7]);
    const float c = 0.70710678118654752440f;
    f2 o1 = D > 0 ? f2{(x[3].x - x[3].y) * c, (x[3].x + x[3].y) * c} : f2{(x[3].x + x[3].y) * c, (x[3].y - x[3].x) * c};
    f2 o2 = muli<D>(x[5]);
    f2 o3 = D > 0 ? f2{(-x[7].x - x[7].y) * c, (x[7].x - x[7].y) * c} : f2{(x[7].y - x[7].x) * c, (-x[7].x - x[7].y) * c};
    f2 o0 = x[1];
    f2 e0 = x[0], e1 = x[2], e2 = x[4], e3 = x[6];
    x[0] = e0 + o0; x[4] = e0 - o0;
    x[1] = e1 + o1; x[5] = e1 - o1;
    x[2] = e2 + o2; x[6] = e2 - o2;
    x[3] = e3 + o3; x[7] = e3 - o3;
}
template <int D> __device__ __forceinline__ void dft16(f2* x) {
    f2 e[8], o[8];
#pragma unroll
    for (int i = 0; i < 8; ++i) { e[i] = x[2 * i]; o[i] = x[2 * i + 1]; }
    dft8<D>(e); dft8<D>(o);
    const float c1 = 0.92387953251128675613f, s1 = 0.38268343236508977173f, c2 = 0.70710678118654752440f;
    o[1] = twm<D>(o[1], f2{c1, -s1});
    o[2] = twm<D>(o[2], f2{c2, -c2});
    o[3] = twm<D>(o[3], f2{s1, -c1});
    o[4] = muli<D>(o[4]);
    o[5] = twm<D>(o[5], f2{-s1, -c1});
    o[6] = twm<D>(o[6], f2{-c2, -c2});
    o[7] = twm<D>(o[7], f2{-c1, -s1});
#pragma unroll
    for (int i = 0; i < 8; ++i) { x[i] = e[i] + o[i]; x[i + 8] = e[i] - o[i]; }
}
template <int N> __device__ __forceinline__ void twpow(f2* w, int num, float inv_den2) {
    float s, c; sincospif(-(float)num * inv_den2, &s, &c);
    w[1] = f2{c, s};
#pragma unroll
    for (int k = 2; k < N; ++k) w[k] = (k & 1) ? cmul(w[k - 1], w[1]) : cmul(w[k / 2], w[k / 2]);
}
__device__ __forceinline__ float wave_sum(float v) {
#pragma unroll
    for (int o = 32; o >= 1; o >>= 1) v += __shfl_xor(v, o);
    return v;
}
__device__ __forceinline__ void ln_row(float* xe, float* xo) {
    float s = 0.f;
#pragma unroll
    for (int m = 0; m < 8; ++m) s += xe[m] + xo[m];
    s = wave_sum(s);
    const float mean = s * (1.0f / 1024.0f);
    float q = 0.f;
#pragma unroll
    for (int m = 0; m < 8; ++m) { float a = xe[m] - mean, b = xo[m] - mean; q += a * a + b * b; }
    q = wave_sum(q);
    const float mb = bfr(mean), vb = bfr(q * (1.0f / 1024.0f));
    const float den = bfr(sqrtf(bfr(vb + 1e-5f)));
#pragma unroll
    for (int m = 0; m < 8; ++m) { xe[m] = bfr(bfr(xe[m] - mb) / den); xo[m] = bfr(bfr(xo[m] - mb) / den); }
}

constexpr int RS = 580;
__global__ __launch_bounds__(512) void k1_ln_fft(const u16* __restrict__ x, f2* __restrict__ P, const u16* __restrict__ wr,
                                                 const u16* __restrict__ wi, u32* __restrict__ Wt, u32* __restrict__ flags) {
    __shared__ __attribute__((aligned(16))) f2 sm[8 * RS];
    const int tid = threadIdx.x, lane = tid & 63, wv = tid >> 6;
    if (blockIdx.x >= 256) {
        const int b = blockIdx.x - 256;
        if (b == 0 && tid < 256) flags[tid * 16] = 0u;
        u32* tile = (u32*)sm;
        const int k0 = (b & 31) * 64, c0 = (b >> 5) * 64;
#pragma unroll
        for (int i = 0; i < 8; ++i) {
            const int r = wv + 8 * i, c = c0 + lane;
            u32 v = 0;
            if (c < DF) v = (u32)wr[(k0 + r) * DF + c] | ((u32)wi[(k0 + r) * DF + c] << 16);
            tile[r * 65 + lane] = v;
        }
        __syncthreads();
#pragma unroll
        for (int i = 0; i < 8; ++i) {
            const int c = wv + 8 * i;
            if (c0 + c < DF) Wt[(c0 + c) * S_ + k0 + lane] = tile[lane * 65 + c];
        }
        return;
    }
    const int s = blockIdx.x * 8 + wv;
    const u32* xr = (const u32*)x + (size_t)s * 512;
    float xe[8], xo[8];
#pragma unroll
    for (int m = 0; m < 8; ++m) { u32 r = xr[lane + 64 * m]; xe[m] = bf2f(r & 0xffffu); xo[m] = bf2f(r >> 16); }
    ln_row(xe, xo);
    f2 v[8], w[8];
#pragma unroll
    for (int m = 0; m < 8; ++m) v[m] = f2{xe[m], xo[m]};
    f2* R = sm + wv * RS;
    dft8<-1>(v);
    twpow<8>(w, lane, 2.0f / 512.0f);
    R[lane] = v[0];
#pragma unroll
    for (int k = 1; k < 8; ++k) R[k * 72 + lane] = cmul(v[k], w[k]);
    __syncthreads();
    {
        const int k1 = lane >> 3, b = lane & 7;
#pragma unroll
        for (int a = 0; a < 8; ++a) v[a] = R[k1 * 72 + 8 * a + b];
        dft8<-1>(v);
        twpow<8>(w, b, 2.0f / 64.0f);
        __syncthreads();
        R[k1 * 72 + b * 9] = v[0];
#pragma unroll
        for (int k = 1; k < 8; ++k) R[k1 * 72 + b * 9 + k] = cmul(v[k], w[k]);
    }
    __syncthreads();
    {
        const int k1 = lane & 7, k2 = lane >> 3;
#pragma unroll
        for (int b = 0; b < 8; ++b) v[b] = R[k1 * 72 + 9 * b + k2];
        dft8<-1>(v);
    }
    __syncthreads();
#pragma unroll
    for (int k = 0; k < 8; ++k) R[lane + 64 * k] = v[k];
    __syncthreads();
    const int s0 = blockIdx.x * 8;
#pragma unroll
    for (int i = 0; i < 4; ++i) {
        const int it = tid + 512 * i, r = it & 7, p = it >> 3;
        const int ca = p, cb = p ? 512 - p : 256;
        f2 a = sm[r * RS + ca], b = sm[r * RS + cb];
        *(f32x4*)(P + ((size_t)p * S_ + s0 + r) * 2) = f32x4{a.x, a.y, b.x, b.y};
    }
}

constexpr int CS = 2192;
__global__ __launch_bounds__(256) void k2_col(f2* __restrict__ P, const u32* __restrict__ Wt) {
    __shared__ __attribute__((aligned(16))) f2 sm[2 * CS];
    const int p = blockIdx.x, tid = threadIdx.x;
    const int c = tid & 1, t = tid >> 1, b = t & 7, k1 = t >> 3;
    f2* base = P + (size_t)p * 4096;
    f2 v[16], w[16];
#pragma unroll
    for (int m = 0; m < 16; ++m) v[m] = base[(128 * m + t) * 2 + c];
    const int T = tid, Tm = (256 - T) & 255;
    const int colA = p, colB = p ? 512 - p : 256;
    u32 wa[8], wb[8];
    if (p) {
#pragma unroll
        for (int j = 0; j < 8; ++j) { wa[j] = Wt[colA * S_ + T + 256 * j]; wb[j] = Wt[colB * S_ + Tm + 256 * j]; }
    }
    dft16<-1>(v);
    twpow<16>(w, t, 2.0f / 2048.0f);
    f2* C = sm + c * CS;
    C[t] = v[0];
#pragma unroll
    for (int k = 1; k < 16; ++k) C[k * 136 + t] = cmul(v[k], w[k]);
    __syncthreads();
#pragma unroll
    for (int a = 0; a < 16; ++a) v[a] = C[k1 * 136 + 8 * a + b];
    dft16<-1>(v);
    twpow<16>(w, b, 2.0f / 128.0f);
    __syncthreads();
    C[b * 258 + k1] = v[0];
#pragma unroll
    for (int k = 1; k < 16; ++k) C[b * 258 + k1 + 16 * k] = cmul(v[k], w[k]);
    __syncthreads();
    f2* A = v; f2* B = v + 8;
#pragma unroll
    for (int j = 0; j < 8; ++j) { A[j] = sm[j * 258 + T]; B[j] = sm[CS + j * 258 + Tm]; }
    dft8<-1>(A); dft8<-1>(B);
    const float NRM = 1.0f / 4194304.0f;
    if (p) {
        float sn, cn; sincospif(-(float)p * (2.0f / 1024.0f), &sn, &cn);
        const f2 tw = f2{cn, sn};
        if (T == 0) {
            f2 t0 = B[0]; u32 w0 = wb[0];
#pragma unroll
            for (int j = 0; j < 7; ++j) { B[j] = B[j + 1]; wb[j] = wb[j + 1]; }
            B[7] = t0; wb[7] = w0;
        }
#pragma unroll
        for (int j = 0; j < 8; ++j) {
            const f2 za = A[j], zb = B[7 - j];
            const f2 E = f2{za.x + zb.x, za.y - zb.y};
            const f2 Dd = f2{za.x - zb.x, za.y + zb.y};
            const f2 O = f2{Dd.y, -Dd.x};
            const f2 wO = cmul(O, tw);
            const f2 Xa = E + wO, Xb = cconj(E - wO);
            const f2 fa = f2{bf2f(wa[j] & 0xffffu) * NRM, bf2f(wa[j] >> 16) * NRM};
            const f2 fb = f2{bf2f(wb[7 - j] & 0xffffu) * NRM, bf2f(wb[7 - j] >> 16) * NRM};
            const f2 Ya = cmul(Xa, fa), Yb = cmul(Xb, fb);
            const f2 E2 = f2{Ya.x + Yb.x, Ya.y - Yb.y};
            const f2 O2 = cmulc(f2{Ya.x - Yb.x, Ya.y + Yb.y}, tw);
            A[j] = f2{E2.x - O2.y, E2.y + O2.x};
            B[7 - j] = f2{E2.x + O2.y, O2.x - E2.y};
        }
        if (T == 0) {
            f2 t7 = B[7];
#pragma unroll
            for (int j = 7; j > 0; --j) B[j] = B[j - 1];
            B[0] = t7;
        }
    } else {
        __syncthreads();
#pragma unroll
        for (int j = 0; j < 8; ++j) { sm[T + 256 * j] = A[j]; sm[CS + Tm + 256 * j] = B[j]; }
        __syncthreads();
#pragma unroll
        for (int j = 0; j < 8; ++j) {
            {
                const int k = T + 256 * j, km = (2048 - k) & 2047;
                const f2 za = sm[k], zb = sm[km];
                const f2 E = f2{za.x + zb.x, za.y - zb.y};
                const f2 Dd = f2{za.x - zb.x, za.y + zb.y};
                const f2 O = f2{Dd.y, -Dd.x};
                const f2 X0 = E + O, X5 = E - O;
                const u32 u0 = Wt[k], u0m = Wt[km], u5 = Wt[512 * S_ + k], u5m = Wt[512 * S_ + km];
                const f2 y0 = cmul(X0, f2{bf2f(u0 & 0xffffu), bf2f(u0 >> 16)}), y0m = cmul(cconj(X0), f2{bf2f(u0m & 0xffffu), bf2f(u0m >> 16)});
                const f2 y5 = cmul(X5, f2{bf2f(u5 & 0xffffu), bf2f(u5 >> 16)}), y5m = cmul(cconj(X5), f2{bf2f(u5m & 0xffffu), bf2f(u5m >> 16)});
                const f2 s0 = f2{(y0.x + y0m.x) * (0.5f * NRM), (y0.y - y0m.y) * (0.5f * NRM)};
                const f2 s5 = f2{(y5.x + y5m.x) * (0.5f * NRM), (y5.y - y5m.y) * (0.5f * NRM)};
                const f2 E2 = s0 + s5, O2 = s0 - s5;
                A[j] = f2{E2.x - O2.y, E2.y + O2.x};
            }
            {
                const int k = Tm + 256 * j, km = (2048 - k) & 2047;
                const f2 za = sm[CS + k], zb = sm[CS + km];
                const f2 E = f2{za.x + zb.x, za.y - zb.y};
                const f2 Dd = f2{za.x - zb.x, za.y + zb.y};
                const f2 O = f2{Dd.y, -Dd.x};
                const f2 tw = f2{0.f, -1.f};
                const f2 wO = cmul(O, tw);
                const f2 Xa = E + wO, Xb = cconj(E - wO);
                const u32 ua = Wt[256 * S_ + k], ub = Wt[256 * S_ + km];
                const f2 Ya = cmul(Xa, f2{bf2f(ua & 0xffffu) * NRM, bf2f(ua >> 16) * NRM});
                const f2 Yb = cmul(Xb, f2{bf2f(ub & 0xffffu) * NRM, bf2f(ub >> 16) * NRM});
                const f2 E2 = f2{Ya.x + Yb.x, Ya.y - Yb.y};
                const f2 O2 = cmulc(f2{Ya.x - Yb.x, Ya.y + Yb.y}, tw);
                B[j] = f2{E2.x - O2.y, E2.y + O2.x};
            }
        }
    }
    dft8<1>(A); dft8<1>(B);
    __syncthreads();
    {
        twpow<8>(w, T >> 4, 2.0f / 128.0f);
        sm[T] = A[0];
#pragma unroll
        for (int j = 1; j < 8; ++j) sm[j * 258 + T] = cmulc(A[j], w[j]);
        twpow<8>(w, Tm >> 4, 2.0f / 128.0f);
        sm[CS + Tm] = B[0];
#pragma unroll
        for (int j = 1; j < 8; ++j) sm[CS + j * 258 + Tm] = cmulc(B[j], w[j]);
    }
    __syncthreads();
#pragma unroll
    for (int k = 0; k < 16; ++k) v[k] = C[b * 258 + k1 + 16 * k];
    dft16<1>(v);
    __syncthreads();
    {
        float sn, cn;
        sincospif(-(float)(b * k1) * (2.0f / 2048.0f), &sn, &cn);
        f2 wc = f2{cn, sn};
        sincospif(-(float)(8 * k1) * (2.0f / 2048.0f), &sn, &cn);
        const f2 ws = f2{cn, sn};
        w[1] = ws;
#pragma unroll
        for (int k = 2; k < 16; ++k) w[k] = (k & 1) ? cmul(w[k - 1], w[1]) : cmul(w[k / 2], w[k / 2]);
        C[k1 * 136 + b] = cmulc(v[0], wc);
#pragma unroll
        for (int a = 1; a < 16; ++a) C[k1 * 136 + 8 * a + b] = cmulc(v[a], cmul(wc, w[a]));
    }
    __syncthreads();
#pragma unroll
    for (int k = 0; k < 16; ++k) v[k] = C[k * 136 + t];
    dft16<1>(v);
#pragma unroll
    for (int m = 0; m < 16; ++m) base[(128 * m + t) * 2 + c] = v[m];
}

__global__ __launch_bounds__(512) void k3_ifft_ln(const f2* __restrict__ P, const u16* __restrict__ x, u16* __restrict__ h, u16* __restrict__ ln2) {
    __shared__ __attribute__((aligned(16))) f2 sm[8 * RS];
    const int tid = threadIdx.x, lane = tid & 63, wv = tid >> 6;
    const int s0 = blockIdx.x * 8, s = s0 + wv;
    const u32* xr = (const u32*)x + (size_t)s * 512;
    u32 xraw[8];
#pragma unroll
    for (int m = 0; m < 8; ++m) xraw[m] = xr[lane + 64 * m];
#pragma unroll
    for (int i = 0; i < 4; ++i) {
        const int it = tid + 512 * i, r = it & 7, p = it >> 3;
        const int ca = p, cb = p ? 512 - p : 256;
        f32x4 q = *(const f32x4*)(P + ((size_t)p * S_ + s0 + r) * 2);
        sm[r * RS + ca] = f2{q[0], q[1]};
        sm[r * RS + cb] = f2{q[2], q[3]};
    }
    __syncthreads();
    f2* R = sm + wv * RS;
    f2 v[8], w[8];
#pragma unroll
    for (int k = 0; k < 8; ++k) v[k] = R[lane + 64 * k];
    dft8<1>(v);
    __syncthreads();
    {
        const int k1 = lane & 7, k2 = lane >> 3;
        twpow<8>(w, k2, 2.0f / 64.0f);
        R[k1 * 72 + k2] = v[0];
#pragma unroll
        for (int b = 1; b < 8; ++b) R[k1 * 72 + 9 * b + k2] = cmulc(v[b], w[b]);
    }
    __syncthreads();
    {
        const int k1 = lane >> 3, b = lane & 7;
#pragma unroll
        for (int k = 0; k < 8; ++k) v[k] = R[k1 * 72 + b * 9 + k];
        dft8<1>(v);
        __syncthreads();
        float sn, cn;
        sincospif(-(float)(b * k1) * (2.0f / 512.0f), &sn, &cn);
        const f2 wc = f2{cn, sn};
        sincospif(-(float)(8 * k1) * (2.0f / 512.0f), &sn, &cn);
        w[1] = f2{cn, sn};
#pragma unroll
        for (int k = 2; k < 8; ++k) w[k] = (k & 1) ? cmul(w[k - 1], w[1]) : cmul(w[k / 2], w[k / 2]);
        R[k1 * 72 + b] = cmulc(v[0], wc);
#pragma unroll
        for (int a = 1; a < 8; ++a) R[k1 * 72 + 8 * a + b] = cmulc(v[a], cmul(wc, w[a]));
    }
    __syncthreads();
#pragma unroll
    for (int k = 0; k < 8; ++k) v[k] = R[k * 72 + lane];
    dft8<1>(v);
    float he[8], ho[8];
    u32* hr = (u32*)h + (size_t)s * 512;
    u32* lr = (u32*)ln2 + (size_t)s * 512;
#pragma unroll
    for (int m = 0; m < 8; ++m) {
        he[m] = bfr(bf2f(xraw[m] & 0xffffu) + bfr(v[m].x));
        ho[m] = bfr(bf2f(xraw[m] >> 16) + bfr(v[m].y));
        hr[lane + 64 * m] = f2bf(he[m]) | (f2bf(ho[m]) << 16);
    }
    ln_row(he, ho);
#pragma unroll
    for (int m = 0; m < 8; ++m) lr[lane + 64 * m] = f2bf(he[m]) | (f2bf(ho[m]) << 16);
}

__device__ __forceinline__ void glds16(const void* g, LAS void* l) { __builtin_amdgcn_global_load_lds((const u32*)g, (LAS u32*)l, 16, 0, 0); }
typedef short v4i16_t __attribute__((ext_vector_type(4)));
__device__ __forceinline__ s16x4 trread(LAS char* p) { return __builtin_bit_cast(s16x4, __builtin_amdgcn_ds_read_tr16_b64_v4i16((LAS v4i16_t*)p)); }
__device__ __forceinline__ bf16x8 cat8(s16x4 a, s16x4 b) { return bf16x8{a[0], a[1], a[2], a[3], b[0], b[1], b[2], b[3]}; }
#define WAITV0() asm volatile("s_waitcnt vmcnt(0)" ::: "memory")

__device__ __forceinline__ float gelu_tanh(float x) {
    const float k0 = 0.7978845608028654f, k1 = 0.044715f;
    const float u = k0 * (x + k1 * x * x * x);
    const float e = __expf(-2.0f * u);
    return x / (1.0f + e);
}

constexpr int G1_A = 32768, G1_B = 28672, G1_ST = G1_A + G1_B;
__global__ __launch_bounds__(512, 2) void k4_gemm1(const u16* __restrict__ A, const u16* __restrict__ Wg, const u16* __restrict__ Wu, u16* __restrict__ act) {
    __shared__ __attribute__((aligned(16))) char smem[2 * G1_ST];
    LAS char* lds = (LAS char*)smem;
    const int tid = threadIdx.x, lane = tid & 63, wv = __builtin_amdgcn_readfirstlane(tid >> 6);
    const int bid = blockIdx.x, xcd = bid & 7, loc = bid >> 3;
    const int mt = (xcd & 1) * 4 + (loc & 3), nt = (xcd >> 1) * 8 + (loc >> 2);
    const int row0 = mt * 256, col0 = nt * 112;
    const char* src[8];
#pragma unroll
    for (int i = 0; i < 4; ++i) {
        const int ch = (wv + 8 * i) * 64 + lane, r = ch >> 3, cp = ch & 7, cc = cp ^ (r & 7);
        src[i] = (const char*)(A + (size_t)(row0 + r) * D_ + cc * 8);
    }
#pragma unroll
    for (int i = 0; i < 4; ++i) {
        int ch = (wv + 8 * i) * 64 + lane; if (ch >= 1792) ch = 0;
        const int blk = ch >> 3, wi = ch & 7, q = wi >> 1, hf = wi & 1;
        const int kb = blk / 14, pos = blk % 14, nb = pos ^ ((kb >> 1) & 1), k = 4 * kb + q;
        const u16* W = nb < 7 ? Wg : Wu;
        const int col = col0 + (nb < 7 ? nb : nb - 7) * 16 + hf * 8;
        src[4 + i] = (const char*)(W + (size_t)k * FF + col);
    }
    auto stage = [&](int buf) {
        LAS char* b = lds + buf * G1_ST;
#pragma unroll
        for (int i = 0; i < 4; ++i) { glds16(src[i], b + (wv + 8 * i) * 1024); src[i] += 128; }
#pragma unroll
        for (int i = 0; i < 4; ++i) {
            if (i < 3 || wv < 4) glds16(src[4 + i], b + G1_A + (wv + 8 * i) * 1024);
            src[4 + i] += (size_t)64 * FF * 2;
        }
    };
    const int fr = lane & 15, fq = lane >> 4;
    int aoff[2];
#pragma unroll
    for (int ks = 0; ks < 2; ++ks) aoff[ks] = (wv * 32 + fr) * 128 + (((ks * 4 + fq) ^ (fr & 7)) << 4);
    const int bl = G1_A + (2 * fq * 14) * 128 + (fr >> 2) * 32 + (fr & 3) * 8;
    const int boffE = bl + (fq & 1) * 128, boffO = bl - (fq & 1) * 128;
    f32x4 acc[2][14];
#pragma unroll
    for (int m = 0; m < 2; ++m)
#pragma unroll
        for (int n = 0; n < 14; ++n) acc[m][n] = f32x4{0.f, 0.f, 0.f, 0.f};
    stage(0);
    constexpr int NT = D_ / 64;
    for (int t = 0; t < NT; ++t) {
        WAITV0();
        __syncthreads();
        if (t + 1 < NT) stage((t + 1) & 1);
        LAS char* b = lds + (t & 1) * G1_ST;
#pragma unroll
        for (int ks = 0; ks < 2; ++ks) {
            bf16x8 a0 = *(LAS bf16x8*)(b + aoff[ks]);
            bf16x8 a1 = *(LAS bf16x8*)(b + aoff[ks] + 16 * 128);
#pragma unroll
            for (int n = 0; n < 14; ++n) {
                LAS char* pb = b + ((n & 1) ? boffO : boffE) + n * 128 + ks * (8 * 14 * 128);
                bf16x8 bf = cat8(trread(pb), trread(pb + 14 * 128));
                acc[0][n] = __builtin_amdgcn_mfma_f32_16x16x32_bf16(bf, a0, acc[0][n], 0, 0, 0);
                acc[1][n] = __builtin_amdgcn_mfma_f32_16x16x32_bf16(bf, a1, acc[1][n], 0, 0, 0);
            }
        }
    }
#pragma unroll
    for (int m = 0; m < 2; ++m) {
        u16* dst = act + (size_t)(row0 + wv * 32 + m * 16 + fr) * FF + col0 + fq * 4;
#pragma unroll
        for (int n = 0; n < 7; ++n) {
            float o[4];
#pragma unroll
            for (int j = 0; j < 4; ++j) {
                const float g = bfr(acc[m][n][j]), u = bfr(acc[m][n + 7][j]);
                o[j] = bfr(gelu_tanh(g)) * u;
            }
            *(u32x2*)(dst + n * 16) = u32x2{pack2(o[0], o[1]), pack2(o[2], o[3])};
        }
    }
}

constexpr int G2_ST = 32768;
__global__ __launch_bounds__(512, 2) void k5_gemm2(const u16* __restrict__ A, const u16* __restrict__ Wd, const u16* __restrict__ h,
                                                   u16* __restrict__ out, f32x4* __restrict__ part, u32* __restrict__ flags) {
    __shared__ __attribute__((aligned(16))) char smem[2 * G2_ST];
    LAS char* lds = (LAS char*)smem;
    const int tid = threadIdx.x, lane = tid & 63, wv = __builtin_amdgcn_readfirstlane(tid >> 6);
    const int bid = blockIdx.x, xcd = bid & 7, loc = bid >> 3;
    const int ksp = xcd & 1, mt = (xcd >> 1) * 4 + (loc & 3), nt = loc >> 2;
    const int row0 = mt * 128, col0 = nt * 128, kbase = ksp * (FF / 2);
    const char* src[4];
#pragma unroll
    for (int i = 0; i < 2; ++i) {
        const int ch = (wv + 8 * i) * 64 + lane, r = ch >> 3, cp = ch & 7, cc = cp ^ (r & 7);
        src[i] = (const char*)(A + (size_t)(row0 + r) * FF + kbase + cc * 8);
    }
#pragma unroll
    for (int i = 0; i < 2; ++i) {
        const int ch = (wv + 8 * i) * 64 + lane, blk = ch >> 3, wi = ch & 7, q = wi >> 1, hf = wi & 1;
        const int kb = blk >> 3, pos = blk & 7, nb = pos ^ ((kb >> 1) & 1), k = 4 * kb + q;
        src[2 + i] = (const char*)(Wd + (size_t)(kbase + k) * D_ + col0 + nb * 16 + hf * 8);
    }
    auto stage = [&](int buf) {
        LAS char* b = lds + buf * G2_ST;
#pragma unroll
        for (int i = 0; i < 2; ++i) { glds16(src[i], b + (wv + 8 * i) * 1024); src[i] += 128; }
#pragma unroll
        for (int i = 0; i < 2; ++i) { glds16(src[2 + i], b + 16384 + (wv + 8 * i) * 1024); src[2 + i] += (size_t)64 * D_ * 2; }
    };
    const int fr = lane & 15, fq = lane >> 4, wr = wv >> 2, wc = wv & 3;
    int aoff[2];
#pragma unroll
    for (int ks = 0; ks < 2; ++ks) aoff[ks] = (wr * 64 + fr) * 128 + (((ks * 4 + fq) ^ (fr & 7)) << 4);
    const int bl = 16384 + (2 * fq * 8) * 128 + (fr >> 2) * 32 + (fr & 3) * 8 + wc * 256;
    const int boffE = bl + (fq & 1) * 128, boffO = bl - (fq & 1) * 128;
    f32x4 acc[4][2];
#pragma unroll
    for (int m = 0; m < 4; ++m)
#pragma unroll
        for (int n = 0; n < 2; ++n) acc[m][n] = f32x4{0.f, 0.f, 0.f, 0.f};
    stage(0);
    constexpr int NT = FF / 2 / 64;
    for (int t = 0; t < NT; ++t) {
        WAITV0();
        __syncthreads();
        if (t + 1 < NT) stage((t + 1) & 1);
        LAS char* b = lds + (t & 1) * G2_ST;
#pragma unroll
        for (int ks = 0; ks < 2; ++ks) {
            bf16x8 bf[2];
#pragma unroll
            for (int n = 0; n < 2; ++n) {
                LAS char* pb = b + ((n & 1) ? boffO : boffE) + n * 128 + ks * (8 * 8 * 128);
                bf[n] = cat8(trread(pb), trread(pb + 8 * 128));
            }
#pragma unroll
            for (int m = 0; m < 4; ++m) {
                bf16x8 a = *(LAS bf16x8*)(b + aoff[ks] + m * 16 * 128);
#pragma unroll
                for (int n = 0; n < 2; ++n) acc[m][n] = __builtin_amdgcn_mfma_f32_16x16x32_bf16(bf[n], a, acc[m][n], 0, 0, 0);
            }
        }
    }
    const int tile = mt * 8 + nt;
    f32x4* mine = part + ((size_t)(tile * 2 + ksp) * 4 + wc) * 8 * 64 + lane;
    const f32x4* theirs = part + ((size_t)(tile * 2 + (ksp ^ 1)) * 4 + wc) * 8 * 64 + lane;
    if (wr != ksp) {
        __amdgpu_buffer_rsrc_t rs = __builtin_amdgcn_make_buffer_rsrc((void*)part, 0, 0x7fffffff, 0x00020000);
        const int off = (int)((char*)mine - (char*)part);
#pragma unroll
        for (int m = 0; m < 4; ++m)
#pragma unroll
            for (int n = 0; n < 2; ++n)
                __builtin_amdgcn_raw_buffer_store_b128(__builtin_bit_cast(u32x4, acc[m][n]), rs, off + (m * 2 + n) * 1024, 0, 16);
    }
    WAITV0();
    __syncthreads();
    GAS u32* fl = (GAS u32*)flags;
    if (tid == 0) __hip_atomic_store(fl + (tile * 2 + ksp) * 16, 1u, __ATOMIC_RELAXED, __HIP_MEMORY_SCOPE_AGENT);
    if (wv == 0) {
        unsigned spins = 0;
        while (__builtin_amdgcn_readfirstlane(__hip_atomic_load(fl + (tile * 2 + (ksp ^ 1)) * 16, __ATOMIC_RELAXED, __HIP_MEMORY_SCOPE_AGENT)) == 0u) {
            __builtin_amdgcn_s_sleep(1);
            if (++spins > (1u << 22)) break;
        }
        __builtin_amdgcn_fence(__ATOMIC_ACQUIRE, "agent");
        WAITV0();
    }
    __syncthreads();
    if (wr == ksp) {
#pragma unroll
        for (int m = 0; m < 4; ++m) {
            const size_t ro = (size_t)(row0 + wr * 64 + m * 16 + fr) * D_ + col0 + wc * 32 + fq * 4;
#pragma unroll
            for (int n = 0; n < 2; ++n) {
                const f32x4 o = theirs[(m * 2 + n) * 64];
                const u32x2 hv = *(const u32x2*)(h + ro + n * 16);
                float r[4];
#pragma unroll
                for (int j = 0; j < 4; ++j) {
                    const float sum = ksp ? (o[j] + acc[m][n][j]) : (acc[m][n][j] + o[j]);
                    const u32 hw = hv[j >> 1];
                    r[j] = bf2f((j & 1) ? (hw >> 16) : (hw & 0xffffu)) + bfr(sum);
                }
                *(u32x2*)(out + ro + n * 16) = u32x2{pack2(r[0], r[1]), pack2(r[2], r[3])};
            }
        }
    }
}

extern "C" void kernel_launch(void* const* d_in, const int* in_sizes, int n_in, void* d_out, int out_size, void* d_ws, size_t ws_size,
                              hipStream_t stream) {
    const u16* x = (const u16*)d_in[0];
    const u16* wr = (const u16*)d_in[1];
    const u16* wi = (const u16*)d_in[2];
    const u16* wg = (const u16*)d_in[4];
    const u16* wu = (const u16*)d_in[5];
    const u16* wd = (const u16*)d_in[6];
    u16* out = (u16*)d_out;
    char* ws = (char*)d_ws;
    f2* P = (f2*)(ws);
    u32* Wt = (u32*)(ws + (8u << 20));
    u16* h = (u16*)(ws + (13u << 20));
    u16* ln2 = (u16*)(ws + (17u << 20));
    u16* act = (u16*)(ws + (21u << 20));
    f32x4* part = (f32x4*)(ws + (36u << 20));
    u32* flags = (u32*)(ws + (44u << 20));
    k1_ln_fft<<<256 + 288, 512, 0, stream>>>(x, P, wr, wi, Wt, flags);
    k2_col<<<256, 256, 0, stream>>>(P, Wt);
    k3_ifft_ln<<<256, 512, 0, stream>>>(P, x, h, ln2);
    k4_gemm1<<<256, 512, 0, stream>>>(ln2, wg, wu, act);
    k5_gemm2<<<256, 512, 0, stream>>>(act, wd, h, out, part, flags);
}
```

```cpp
#include <hip/hip_runtime.h>
#include <hip/hip_bf16.h>

typedef unsigned short u16;
typedef unsigned int u32;
typedef float f2 __attribute__((ext_vector_type(2)));
typedef float f32x4 __attribute__((ext_vector_type(4)));
typedef short bf16x8 __attribute__((ext_vector_type(8)));
typedef short s16x4 __attribute__((ext_vector_type(4)));
typedef unsigned u32x4 __attribute__((ext_vector_type(4)));
typedef unsigned u32x2 __attribute__((ext_vector_type(2)));
#define LAS __attribute__((address_space(3)))
#define GAS __attribute__((address_space(1)))

constexpr int S_ = 2048, D_ = 1024, DF = 513, FF = 3584;

__device__ __forceinline__ float bf2f(u32 h) { return __uint_as_float(h << 16); }
__device__ __forceinline__ u32 f2bf(float f) { __bf16 b = (__bf16)f; return (u32)__builtin_bit_cast(u16, b); }
__device__ __forceinline__ float bfr(float f) { return bf2f(f2bf(f)); }
typedef __bf16 bf16x2_t __attribute__((ext_vector_type(2)));
__device__ __forceinline__ u32 pack2(float lo, float hi) { return __builtin_bit_cast(u32, __builtin_convertvector(f2{lo, hi}, bf16x2_t)); }

__device__ __forceinline__ f2 cmul(f2 a, f2 b) { return f2{a.x * b.x - a.y * b.y, a.x * b.y + a.y * b.x}; }
__device__ __forceinline__ f2 cmulc(f2 a, f2 b) { return f2{a.x * b.x + a.y * b.y, a.y * b.x - a.x * b.y}; }
__device__ __forceinline__ f2 cconj(f2 a) { return f2{a.x, -a.y}; }
template <int D> __device__ __forceinline__ f2 muli(f2 a) { return D > 0 ? f2{-a.y, a.x} : f2{a.y, -a.x}; }
template <int D> __device__ __forceinline__ f2 twm(f2 a, f2 w) { return D > 0 ? cmulc(a, w) : cmul(a, w); }

template <int D> __device__ __forceinline__ void dft4(f2& x0, f2& x1, f2& x2, f2& x3) {
    f2 t0 = x0 + x2, t1 = x0 - x2, t2 = x1 + x3, t3 = muli<D>(x1 - x3);
    x0 = t0 + t2; x2 = t0 - t2; x1 = t1 + t3; x3 = t1 - t3;
}
template <int D> __device__ __forceinline__ void dft8(f2* x) {
    dft4<D>(x[0], x[2], x[4], x[6]);
    dft4<D>(x[1], x[3], x[5], x[7]);
    const float c = 0.70710678118654752440f;
    f2 o1 = D > 0 ? f2{(x[3].x - x[3].y) * c, (x[3].x + x[3].y) * c} : f2{(x[3].x + x[3].y) * c, (x[3].y - x[3].x) * c};
    f2 o2 = muli<D>(x[5]);
    f2 o3 = D > 0 ? f2{(-x[7].x - x[7].y) * c, (x[7].x - x[7].y) * c} : f2{(x[7].y - x[7].x) * c, (-x[7].x - x[7].y) * c};
    f2 o0 = x[1];
    f2 e0 = x[0], e1 = x[2], e2 = x[4], e3 = x[6];
    x[0] = e0 + o0; x[4] = e0 - o0;
    x[1] = e1 + o1; x[5] = e1 - o1;
    x[2] = e2 + o2; x[6] = e2 - o2;
    x[3] = e3 + o3; x[7] = e3 - o3;
}
template <int D> __device__ __forceinline__ void dft16(f2* x) {
    f2 e[8], o[8];
#pragma unroll
    for (int i = 0; i < 8; ++i) { e[i] = x[2 * i]; o[i] = x[2 * i + 1]; }
    dft8<D>(e); dft8<D>(o);
    const float c1 = 0.92387953251128675613f, s1 = 0.38268343236508977173f, c2 = 0.70710678118654752440f;
    o[1] = twm<D>(o[1], f2{c1, -s1});
    o[2] = twm<D>(o[2], f2{c2, -c2});
    o[3] = twm<D>(o[3], f2{s1, -c1});
    o[4] = muli<D>(o[4]);
    o[5] = twm<D>(o[5], f2{-s1, -c1});
    o[6] = twm<D>(o[6], f2{-c2, -c2});
    o[7] = twm<D>(o[7], f2{-c1, -s1});
#pragma unroll
    for (int i = 0; i < 8; ++i) { x[i] = e[i] + o[i]; x[i + 8] = e[i] - o[i]; }
}
template <int N> __device__ __forceinline__ void twpow(f2* w, int num, float inv_den2) {
    float s, c; sincospif(-(float)num * inv_den2, &s, &c);
    w[1] = f2{c, s};
#pragma unroll
    for (int k = 2; k < N; ++k) w[k] = (k & 1) ? cmul(w[k - 1], w[1]) : cmul(w[k / 2], w[k / 2]);
}
__device__ __forceinline__ float wave_sum(float v) {
#pragma unroll
    for (int o = 32; o >= 1; o >>= 1) v += __shfl_xor(v, o);
    return v;
}
__device__ __forceinline__ void ln_row(float* xe, float* xo) {
    float s = 0.f;
#pragma unroll
    for (int m = 0; m < 8; ++m) s += xe[m] + xo[m];
    s = wave_sum(s);
    const float mean = s * (1.0f / 1024.0f);
    float q = 0.f;
#pragma unroll
    for (int m = 0; m < 8; ++m) { float a = xe[m] - mean, b = xo[m] - mean; q += a * a + b * b; }
    q = wave_sum(q);
    const float mb = bfr(mean), vb = bfr(q * (1.0f / 1024.0f));
    const float den = bfr(sqrtf(bfr(vb + 1e-5f)));
#pragma unroll
    for (int m = 0; m < 8; ++m) { xe[m] = bfr(bfr(xe[m] - mb) / den); xo[m] = bfr(bfr(xo[m] - mb) / den); }
}

constexpr int RS = 580;
__global__ __launch_bounds__(512) void k1_ln_fft(const u16* __restrict__ x, f2* __restrict__ P, const u16* __restrict__ wr,
                                                 const u16* __restrict__ wi, u32* __restrict__ Wt, u32* __restrict__ flags) {
    __shared__ __attribute__((aligned(16))) f2 sm[8 * RS];
    const int tid = threadIdx.x, lane = tid & 63, wv = tid >> 6;
    if (blockIdx.x >= 256) {
        const int b = blockIdx.x - 256;
        if (b == 0 && tid < 256) flags[tid * 16] = 0u;
        u32* tile = (u32*)sm;
        const int k0 = (b & 31) * 64, c0 = (b >> 5) * 64;
#pragma unroll
        for (int i = 0; i < 8; ++i) {
            const int r = wv + 8 * i, c = c0 + lane;
            u32 v = 0;
            if (c < DF) v = (u32)wr[(k0 + r) * DF + c] | ((u32)wi[(k0 + r) * DF + c] << 16);
            tile[r * 65 + lane] = v;
        }
        __syncthreads();
#pragma unroll
        for (int i = 0; i < 8; ++i) {
            const int c = wv + 8 * i;
            if (c0 + c < DF) Wt[(c0 + c) * S_ + k0 + lane] = tile[lane * 65 + c];
        }
        return;
    }
    const int s = blockIdx.x * 8 + wv;
    const u32* xr = (const u32*)x + (size_t)s * 512;
    float xe[8], xo[8];
#pragma unroll
    for (int m = 0; m < 8; ++m) { u32 r = xr[lane + 64 * m]; xe[m] = bf2f(r & 0xffffu); xo[m] = bf2f(r >> 16); }
    ln_row(xe, xo);
    f2 v[8], w[8];
#pragma unroll
    for (int m = 0; m < 8; ++m) v[m] = f2{xe[m], xo[m]};
    f2* R = sm + wv * RS;
    dft8<-1>(v);
    twpow<8>(w, lane, 2.0f / 512.0f);
    R[lane] = v[0];
#pragma unroll
    for (int k = 1; k < 8; ++k) R[k * 72 + lane] = cmul(v[k], w[k]);
    __syncthreads();
    {
        const int k1 = lane >> 3, b = lane & 7;
#pragma unroll
        for (int a = 0; a < 8; ++a) v[a] = R[k1 * 72 + 8 * a + b];
        dft8<-1>(v);
        twpow<8>(w, b, 2.0f / 64.0f);
        __syncthreads();
        R[k1 * 72 + b * 9] = v[0];
#pragma unroll
        for (int k = 1; k < 8; ++k) R[k1 * 72 + b * 9 + k] = cmul(v[k], w[k]);
    }
    __syncthreads();
    {
        const int k1 = lane & 7, k2 = lane >> 3;
#pragma unroll
        for (int b = 0; b < 8; ++b) v[b] = R[k1 * 72 + 9 * b + k2];
        dft8<-1>(v);
    }
    __syncthreads();
#pragma unroll
    for (int k = 0; k < 8; ++k) R[lane + 64 * k] = v[k];
    __syncthreads();
    const int s0 = blockIdx.x * 8;
#pragma unroll
    for (int i = 0; i < 4; ++i) {
        const int it = tid + 512 * i, r = it & 7, p = it >> 3;
        const int ca = p, cb = p ? 512 - p : 256;
        f2 a = sm[r * RS + ca], b = sm[r * RS + cb];
        *(f32x4*)(P + ((size_t)p * S_ + s0 + r) * 2) = f32x4{a.x, a.y, b.x, b.y};
    }
}

constexpr int CS = 2192;
__global__ __launch_bounds__(256) void k2_col(f2* __restrict__ P, const u32* __restrict__ Wt) {
    __shared__ __attribute__((aligned(16))) f2 sm[2 * CS];
    const int p = blockIdx.x, tid = threadIdx.x;
    const int c = tid & 1, t = tid >> 1, b = t & 7, k1 = t >> 3;
    f2* base = P + (size_t)p * 4096;
    f2 v[16], w[16];
#pragma unroll
    for (int m = 0; m < 16; ++m) v[m] = base[(128 * m + t) * 2 + c];
    const int T = tid, Tm = (256 - T) & 255;
    const int colA = p, colB = p ? 512 - p : 256;
    u32 wa[8], wb[8];
    if (p) {
#pragma unroll
        for (int j = 0; j < 8; ++j) { wa[j] = Wt[colA * S_ + T + 256 * j]; wb[j] = Wt[colB * S_ + Tm + 256 * j]; }
    }
    dft16<-1>(v);
    twpow<16>(w, t, 2.0f / 2048.0f);
    f2* C = sm + c * CS;
    C[t] = v[0];
#pragma unroll
    for (int k = 1; k < 16; ++k) C[k * 136 + t] = cmul(v[k], w[k]);
    __syncthreads();
#pragma unroll
    for (int a = 0; a < 16; ++a) v[a] = C[k1 * 136 + 8 * a + b];
    dft16<-1>(v);
    twpow<16>(w, b, 2.0f / 128.0f);
    __syncthreads();
    C[b * 258 + k1] = v[0];
#pragma unroll
    for (int k = 1; k < 16; ++k) C[b * 258 + k1 + 16 * k] = cmul(v[k], w[k]);
    __syncthreads();
    f2* A = v; f2* B = v + 8;
#pragma unroll
    for (int j = 0; j < 8; ++j) { A[j] = sm[j * 258 + T]; B[j] = sm[CS + j * 258 + Tm]; }
    dft8<-1>(A); dft8<-1>(B);
    const float NRM = 1.0f / 4194304.0f;
    if (p) {
        float sn, cn; sincospif(-(float)p * (2.0f / 1024.0f), &sn, &cn);
        const f2 tw = f2{cn, sn};
        if (T == 0) {
            f2 t0 = B[0]; u32 w0 = wb[0];
#pragma unroll
            for (int j = 0; j < 7; ++j) { B[j] = B[j + 1]; wb[j] = wb[j + 1]; }
            B[7] = t0; wb[7] = w0;
        }
#pragma unroll
        for (int j = 0; j < 8; ++j) {
            const f2 za = A[j], zb = B[7 - j];
            const f2 E = f2{za.x + zb.x, za.y - zb.y};
            const f2 Dd = f2{za.x - zb.x, za.y + zb.y};
            const f2 O = f2{Dd.y, -Dd.x};
            const f2 wO = cmul(O, tw);
            const f2 Xa = E + wO, Xb = cconj(E - wO);
            const f2 fa = f2{bf2f(wa[j] & 0xffffu) * NRM, bf2f(wa[j] >> 16) * NRM};
            const f2 fb = f2{bf2f(wb[7 - j] & 0xffffu) * NRM, bf2f(wb[7 - j] >> 16) * NRM};
            const f2 Ya = cmul(Xa, fa), Yb = cmul(Xb, fb);
            const f2 E2 = f2{Ya.x + Yb.x, Ya.y - Yb.y};
            const f2 O2 = cmulc(f2{Ya.x - Yb.x, Ya.y + Yb.y}, tw);
            A[j] = f2{E2.x - O2.y, E2.y + O2.x};
            B[7 - j] = f2{E2.x + O2.y, O2.x - E2.y};
        }
        if (T == 0) {
            f2 t7 = B[7];
#pragma unroll
            for (int j = 7; j > 0; --j) B[j] = B[j - 1];
            B[0] = t7;
        }
    } else {
        __syncthreads();
#pragma unroll
        for (int j = 0; j < 8; ++j) { sm[T + 256 * j] = A[j]; sm[CS + Tm + 256 * j] = B[j]; }
        __syncthreads();
#pragma unroll
        for (int j = 0; j < 8; ++j) {
            {
                const int k = T + 256 * j, km = (2048 - k) & 2047;
                const f2 za = sm[k], zb = sm[km];
                const f2 E = f2{za.x + zb.x, za.y - zb.y};
                const f2 Dd = f2{za.x - zb.x, za.y + zb.y};
                const f2 O = f2{Dd.y, -Dd.x};
                const f2 X0 = E + O, X5 = E - O;
                const u32 u0 = Wt[k], u0m = Wt[km], u5 = Wt[512 * S_ + k], u5m = Wt[512 * S_ + km];
                const f2 y0 = cmul(X0, f2{bf2f(u0 & 0xffffu), bf2f(u0 >> 16)}), y0m = cmul(cconj(X0), f2{bf2f(u0m & 0xffffu), bf2f(u0m >> 16)});
                const f2 y5 = cmul(X5, f2{bf2f(u5 & 0xffffu), bf2f(u5 >> 16)}), y5m = cmul(cconj(X5), f2{bf2f(u5m & 0xffffu), bf2f(u5m >> 16)});
                const f2 s0 = f2{(y0.x + y0m.x) * (0.5f * NRM), (y0.y - y0m.y) * (0.5f * NRM)};
                const f2 s5 = f2{(y5.x + y5m.x) * (0.5f * NRM), (y5.y - y5m.y) * (0.5f * NRM)};
                const f2 E2 = s0 + s5, O2 = s0 - s5;
                A[j] = f2{E2.x - O2.y, E2.y + O2.x};
            }
            {
                const int k = Tm + 256 * j, km = (2048 - k) & 2047;
                const f2 za = sm[CS + k], zb = sm[CS + km];
                const f2 E = f2{za.x + zb.x, za.y - zb.y};
                const f2 Dd = f2{za.x - zb.x, za.y + zb.y};
                const f2 O = f2{Dd.y, -Dd.x};
                const f2 tw = f2{0.f, -1.f};
                const f2 wO = cmul(O, tw);
                const f2 Xa = E + wO, Xb = cconj(E - wO);
                const u32 ua = Wt[256 * S_ + k], ub = Wt[256 * S_ + km];
                const f2 Ya = cmul(Xa, f2{bf2f(ua & 0xffffu) * NRM, bf2f(ua >> 16) * NRM});
                const f2 Yb = cmul(Xb, f2{bf2f(ub & 0xffffu) * NRM, bf2f(ub >> 16) * NRM});
                const f2 E2 = f2{Ya.x + Yb.x, Ya.y - Yb.y};
                const f2 O2 = cmulc(f2{Ya.x - Yb.x, Ya.y + Yb.y}, tw);
                B[j] = f2{E2.x - O2.y, E2.y + O2.x};
            }
        }
    }
    dft8<1>(A); dft8<1>(B);
    __syncthreads();
    {
        twpow<8>(w, T >> 4, 2.0f / 128.0f);
        sm[T] = A[0];
#pragma unroll
        for (int j = 1; j < 8; ++j) sm[j * 258 + T] = cmulc(A[j], w[j]);
        twpow<8>(w, Tm >> 4, 2.0f / 128.0f);
        sm[CS + Tm] = B[0];
#pragma unroll
        for (int j = 1; j < 8; ++j) sm[CS + j * 258 + Tm] = cmulc(B[j], w[j]);
    }
    __syncthreads();
#pragma unroll
    for (int k = 0; k < 16; ++k) v[k] = C[b * 258 + k1 + 16 * k];
    dft16<1>(v);
    __syncthreads();
    {
        float sn, cn;
        sincospif(-(float)(b * k1) * (2.0f / 2048.0f), &sn, &cn);
        f2 wc = f2{cn, sn};
        sincospif(-(float)(8 * k1) * (2.0f / 2048.0f), &sn, &cn);
        const f2 ws = f2{cn, sn};
        w[1] = ws;
#pragma unroll
        for (int k = 2; k < 16; ++k) w[k] = (k & 1) ? cmul(w[k - 1], w[1]) : cmul(w[k / 2], w[k / 2]);
        C[k1 * 136 + b] = cmulc(v[0], wc);
#pragma unroll
        for (int a = 1; a < 16; ++a) C[k1 * 136 + 8 * a + b] = cmulc(v[a], cmul(wc, w[a]));
    }
    __syncthreads();
#pragma unroll
    for (int k = 0; k < 16; ++k) v[k] = C[k * 136 + t];
    dft16<1>(v);
#pragma unroll
    for (int m = 0; m < 16; ++m) base[(128 * m + t) * 2 + c] = v[m];
}

__global__ __launch_bounds__(512) void k3_ifft_ln(const f2* __restrict__ P, const u16* __restrict__ x, u16* __restrict__ h, u16* __restrict__ ln2) {
    __shared__ __attribute__((aligned(16))) f2 sm[8 * RS];
    const int tid = threadIdx.x, lane = tid & 63, wv = tid >> 6;
    const int s0 = blockIdx.x * 8, s = s0 + wv;
    const u32* xr = (const u32*)x + (size_t)s * 512;
    u32 xraw[8];
#pragma unroll
    for (int m = 0; m < 8; ++m) xraw[m] = xr[lane + 64 * m];
#pragma unroll
    for (int i = 0; i < 4; ++i) {
        const int it = tid + 512 * i, r = it & 7, p = it >> 3;
        const int ca = p, cb = p ? 512 - p : 256;
        f32x4 q = *(const f32x4*)(P + ((size_t)p * S_ + s0 + r) * 2);
        sm[r * RS + ca] = f2{q[0], q[1]};
        sm[r * RS + cb] = f2{q[2], q[3]};
    }
    __syncthreads();
    f2* R = sm + wv * RS;
    f2 v[8], w[8];
#pragma unroll
    for (int k = 0; k < 8; ++k) v[k] = R[lane + 64 * k];
    dft8<1>(v);
    __syncthreads();
    {
        const int k1 = lane & 7, k2 = lane >> 3;
        twpow<8>(w, k2, 2.0f / 64.0f);
        R[k1 * 72 + k2] = v[0];
#pragma unroll
        for (int b = 1; b < 8; ++b) R[k1 * 72 + 9 * b + k2] = cmulc(v[b], w[b]);
    }
    __syncthreads();
    {
        const int k1 = lane >> 3, b = lane & 7;
#pragma unroll
        for (int k = 0; k < 8; ++k) v[k] = R[k1 * 72 + b * 9 + k];
        dft8<1>(v);
        __syncthreads();
        float sn, cn;
        sincospif(-(float)(b * k1) * (2.0f / 512.0f), &sn, &cn);
        const f2 wc = f2{cn, sn};
        sincospif(-(float)(8 * k1) * (2.0f / 512.0f), &sn, &cn);
        w[1] = f2{cn, sn};
#pragma unroll
        for (int k = 2; k < 8; ++k) w[k] = (k & 1) ? cmul(w[k - 1], w[1]) : cmul(w[k / 2], w[k / 2]);
        R[k1 * 72 + b] = cmulc(v[0], wc);
#pragma unroll
        for (int a = 1; a < 8; ++a) R[k1 * 72 + 8 * a + b] = cmulc(v[a], cmul(wc, w[a]));
    }
    __syncthreads();
#pragma unroll
    for (int k = 0; k < 8; ++k) v[k] = R[k * 72 + lane];
    dft8<1>(v);
    float he[8], ho[8];
    u32* hr = (u32*)h + (size_t)s * 512;
    u32* lr = (u32*)ln2 + (size_t)s * 512;
#pragma unroll
    for (int m = 0; m < 8; ++m) {
        he[m] = bfr(bf2f(xraw[m] & 0xffffu) + bfr(v[m].x));
        ho[m] = bfr(bf2f(xraw[m] >> 16) + bfr(v[m].y));
        hr[lane + 64 * m] = f2bf(he[m]) | (f2bf(ho[m]) << 16);
    }
    ln_row(he, ho);
#pragma unroll
    for (int m = 0; m < 8; ++m) lr[lane + 64 * m] = f2bf(he[m]) | (f2bf(ho[m]) << 16);
}

__device__ __forceinline__ void glds16(const void* g, LAS void* l) {
    unsigned sv; const unsigned lb = (unsigned)__builtin_amdgcn_readfirstlane((unsigned)(size_t)l);
    asm volatile("s_mov_b32 %0, m0\n\ts_mov_b32 m0, %2\n\ts_nop 0\n\tglobal_load_lds_dwordx4 %1, off\n\ts_mov_b32 m0, %0" : "=&s"(sv) : "v"(g), "s"(lb) : "memory");
}
typedef short v4i16_t __attribute__((ext_vector_type(4)));
__device__ __forceinline__ s16x4 trread(LAS char* p) { return __builtin_bit_cast(s16x4, __builtin_amdgcn_ds_read_tr16_b64_v4i16((LAS v4i16_t*)p)); }
__device__ __forceinline__ bf16x8 cat8(s16x4 a, s16x4 b) { return bf16x8{a[0], a[1], a[2], a[3], b[0], b[1], b[2], b[3]}; }
#define WAITV0() asm volatile("s_waitcnt vmcnt(0)" ::: "memory")

__device__ __forceinline__ float gelu_tanh(float x) {
    const float k0 = 0.7978845608028654f, k1 = 0.044715f;
    const float u = k0 * (x + k1 * x * x * x);
    const float e = __expf(-2.0f * u);
    return x / (1.0f + e);
}

__device__ __forceinline__ f2 geglu2(f2 g, f2 u) {
    const float c0 = -2.0f * 0.7978845608028654f * 1.4426950408889634f, c1 = c0 * 0.044715f;
    const f2 z = g * g;
    const f2 t = g * (z * c1 + c0);
    f2 e; e.x = __builtin_amdgcn_exp2f(t.x); e.y = __builtin_amdgcn_exp2f(t.y);
    const f2 d = e + 1.0f;
    f2 r; r.x = __builtin_amdgcn_rcpf(d.x); r.y = __builtin_amdgcn_rcpf(d.y);
    return (g * r) * u;
}
__device__ __forceinline__ f32x4 geglu4(f32x4 g, f32x4 u) {
    const f2 a = geglu2(f2{g[0], g[1]}, f2{u[0], u[1]}), b = geglu2(f2{g[2], g[3]}, f2{u[2], u[3]});
    return f32x4{a.x, a.y, b.x, b.y};
}

#define BAR() do { asm volatile("s_waitcnt lgkmcnt(0)" ::: "memory"); __builtin_amdgcn_s_barrier(); __builtin_amdgcn_sched_barrier(0); } while (0)
#define WAITV(n) asm volatile("s_waitcnt vmcnt(" #n ")" ::: "memory")
constexpr int G1_AB = 32768, G1_BB = 28672, G1_BOFF = 2 * G1_AB, NT1 = D_ / 64;
__global__ __launch_bounds__(512, 2) void k4_gemm1(const u16* __restrict__ A, const u16* __restrict__ Wg, const u16* __restrict__ Wu, u16* __restrict__ act) {
    __shared__ __attribute__((aligned(16))) char smem[2 * G1_AB + 3 * G1_BB];
    LAS char* lds = (LAS char*)smem;
    const int tid = threadIdx.x, lane = tid & 63, wv = __builtin_amdgcn_readfirstlane(tid >> 6);
    const int bid = blockIdx.x, xcd = bid & 7, loc = bid >> 3;
    const int mt = (xcd & 1) * 4 + (loc & 3), nt = (xcd >> 1) * 8 + (loc >> 2);
    const int row0 = mt * 256, col0 = nt * 112;
    const int grp = wv >> 2, w4 = wv & 3;
    const char* src[8];
    size_t kadv;
    if (grp == 0) {
#pragma unroll
        for (int j = 0; j < 8; ++j) {
            const int pc = w4 + 4 * j, r = pc * 8 + (lane >> 3), cp = lane & 7, cc = cp ^ (r & 7);
            src[j] = (const char*)(A + (size_t)(row0 + r) * D_ + cc * 8);
        }
        kadv = 128;
    } else {
#pragma unroll
        for (int j = 0; j < 8; ++j) {
            int pc = w4 + 4 * j; if (pc >= 28) pc = 0;
            const int ch = pc * 64 + lane, blk = ch >> 3, wi = ch & 7, q = wi >> 1, hf = wi & 1;
            const int kb = blk / 14, pos = blk % 14, nb = pos ^ ((kb >> 1) & 1), k = 4 * kb + q;
            src[j] = (const char*)((hf ? Wu : Wg) + (size_t)k * FF + col0 + nb * 8);
        }
        kadv = (size_t)64 * FF * 2;
    }
    auto stageA = [&](int slot) {
        LAS char* b = lds + slot * G1_AB + w4 * 1024;
#pragma unroll
        for (int j = 0; j < 8; ++j) { glds16(src[j], b + j * 4096); src[j] += kadv; }
    };
    auto stageB = [&](int slot) {
        LAS char* b = lds + G1_BOFF + slot * G1_BB + w4 * 1024;
#pragma unroll
        for (int j = 0; j < 7; ++j) { glds16(src[j], b + j * 4096); src[j] += kadv; }
    };
    const int fr = lane & 15, fq = lane >> 4;
    const int aoff0 = (w4 * 64 + fr) * 128 + ((fq ^ (fr & 7)) << 4);
    const int aoff1 = (w4 * 64 + fr) * 128 + (((4 + fq) ^ (fr & 7)) << 4);
    const int bl = G1_BOFF + (2 * fq * 14) * 128 + (fr >> 2) * 32 + (fr & 3) * 8 + grp * 7 * 128;
    const int sgn = (fq & 1) * 128;
    const int bo0 = bl + (grp ? -sgn : sgn), bo1 = bl + (grp ? sgn : -sgn);
    f32x4 acc[4][7];
#pragma unroll
    for (int m = 0; m < 4; ++m)
#pragma unroll
        for (int n = 0; n < 7; ++n) acc[m][n] = f32x4{0.f, 0.f, 0.f, 0.f};
    bf16x8 af[2][4], bfr_[2][7];
    auto ldfr = [&](int aslot, int bslot) {
        LAS char* b = lds + bslot * G1_BB;
        LAS char* a = lds + aslot * G1_AB;
#pragma unroll
        for (int ks = 0; ks < 2; ++ks) {
#pragma unroll
            for (int n = 0; n < 7; ++n) {
                LAS char* pb = b + ((n & 1) ? bo1 : bo0) + n * 128 + ks * (8 * 14 * 128);
                bfr_[ks][n] = cat8(trread(pb), trread(pb + 14 * 128));
            }
#pragma unroll
            for (int m = 0; m < 4; ++m) af[ks][m] = *(LAS bf16x8*)(a + (ks ? aoff1 : aoff0) + m * 2048);
        }
    };
    auto mma = [&]() {
        __builtin_amdgcn_s_setprio(1);
#pragma unroll
        for (int ks = 0; ks < 2; ++ks)
#pragma unroll
            for (int n = 0; n < 7; ++n)
#pragma unroll
                for (int m = 0; m < 4; ++m) acc[m][n] = __builtin_amdgcn_mfma_f32_16x16x32_bf16(bfr_[ks][n], af[ks][m], acc[m][n], 0, 0, 0);
        __builtin_amdgcn_s_setprio(0);
    };
    if (grp == 0) { stageA(0); WAITV(0); }
    else { stageB(0); stageB(1); WAITV(7); }
    BAR();
    if (grp == 0) {
        int bs = 0;
        for (int t = 0; t < NT1; ++t) {
            if (t + 1 < NT1) stageA((t + 1) & 1);
            ldfr(t & 1, bs);
            BAR();
            mma();
            WAITV(0);
            BAR();
            bs = bs == 2 ? 0 : bs + 1;
        }
    } else {
        BAR();
        int bs = 0;
        for (int t = 0; t < NT1; ++t) {
            const int b2 = bs == 0 ? 2 : bs - 1;
            if (t + 2 < NT1) stageB(b2);
            ldfr(t & 1, bs);
            if (t + 2 < NT1) { WAITV(7); } else { WAITV(0); }
            BAR();
            mma();
            if (t + 1 < NT1) BAR();
            bs = bs == 2 ? 0 : bs + 1;
        }
    }
    {
        char* ob = (char*)act + ((size_t)((row0 >> 4) + w4 * 4) * (FF / 8) + (col0 >> 3) + grp * 7) * 256 + fr * 16;
        auto grp4 = [&](f32x4& t0, f32x4& t1, f32x4& t2, f32x4& t3, size_t o0, size_t o1, size_t o2, size_t o3) {
            float gp[4], up[4], gq[4], uq[4];
#pragma unroll
            for (int r = 0; r < 4; ++r) {
                auto x = __builtin_amdgcn_permlane32_swap(__float_as_uint(t0[r]), __float_as_uint(t2[r]), false, false);
                gp[r] = __uint_as_float(x[0]); up[r] = __uint_as_float(x[1]);
                auto y = __builtin_amdgcn_permlane32_swap(__float_as_uint(t1[r]), __float_as_uint(t3[r]), false, false);
                gq[r] = __uint_as_float(y[0]); uq[r] = __uint_as_float(y[1]);
            }
            const f2 pa = geglu2(f2{gp[0], gp[1]}, f2{up[0], up[1]}), pb = geglu2(f2{gp[2], gp[3]}, f2{up[2], up[3]});
            const f2 qa = geglu2(f2{gq[0], gq[1]}, f2{uq[0], uq[1]}), qb = geglu2(f2{gq[2], gq[3]}, f2{uq[2], uq[3]});
            const u32 p0 = pack2(pa.x, pa.y), p1 = pack2(pb.x, pb.y), q0 = pack2(qa.x, qa.y), q1 = pack2(qb.x, qb.y);
            auto s0 = __builtin_amdgcn_permlane16_swap(p0, q0, false, false);
            auto s1 = __builtin_amdgcn_permlane16_swap(p1, q1, false, false);
            const size_t o = fq == 0 ? o0 : fq == 1 ? o1 : fq == 2 ? o2 : o3;
            *(u32x4*)(ob + o) = u32x4{s0[0], s1[0], s0[1], s1[1]};
        };
        constexpr size_t MS = (size_t)(FF / 8) * 256;
#pragma unroll
        for (int m = 0; m < 4; ++m)
            grp4(acc[m][0], acc[m][1], acc[m][2], acc[m][3], m * MS, m * MS + 256, m * MS + 512, m * MS + 768);
#pragma unroll
        for (int n = 4; n < 7; ++n)
            grp4(acc[0][n], acc[1][n], acc[2][n], acc[3][n], n * 256, MS + n * 256, 2 * MS + n * 256, 3 * MS + n * 256);
    }
}

constexpr int G2_AB = 32768, G2_BB = 16384, G2_BOFF = 2 * G2_AB, NT2 = FF / 4 / 64;
__global__ __launch_bounds__(512, 2) void k5_gemm2(const u16* __restrict__ A, const u16* __restrict__ Wd, const u16* __restrict__ h,
                                                   u16* __restrict__ out, f32x4* __restrict__ part, u32* __restrict__ flags) {
    __shared__ __attribute__((aligned(16))) char smem[2 * G2_AB + 3 * G2_BB];
    LAS char* lds = (LAS char*)smem;
    const int tid = threadIdx.x, lane = tid & 63, wv = __builtin_amdgcn_readfirstlane(tid >> 6);
    const int bid = blockIdx.x, xcd = bid & 7, loc = bid >> 3;
    const int mt = xcd, nt = loc & 7, ksp = loc >> 3;
    const int row0 = mt * 256, col0 = nt * 128, kbase = ksp * (FF / 4);
    const int grp = wv >> 2, w4 = wv & 3;
    const char* src[8];
    size_t kadv;
    if (grp == 0) {
#pragma unroll
        for (int j = 0; j < 8; ++j) {
            const int pc = w4 + 4 * j, r = pc * 8 + (lane >> 3), cp = lane & 7, cc = cp ^ (r & 7);
            src[j] = (const char*)A + ((size_t)((row0 + r) >> 4) * (FF / 8) + (kbase >> 3) + cc) * 256 + (r & 15) * 16;
        }
        kadv = 8 * 256;
    } else {
#pragma unroll
        for (int j = 0; j < 4; ++j) {
            const int pc = w4 + 4 * j, ch = pc * 64 + lane, blk = ch >> 3, wi = ch & 7, q = wi >> 1, hf = wi & 1;
            const int kb = blk >> 3, pos = blk & 7, nb = pos ^ ((kb >> 1) & 1), k = 4 * kb + q;
            src[j] = (const char*)(Wd + (size_t)(kbase + k) * D_ + col0 + nb * 16 + hf * 8);
        }
#pragma unroll
        for (int j = 4; j < 8; ++j) src[j] = nullptr;
        kadv = (size_t)64 * D_ * 2;
    }
    auto stageA = [&](int slot) {
        LAS char* b = lds + slot * G2_AB + w4 * 1024;
#pragma unroll
        for (int j = 0; j < 8; ++j) { glds16(src[j], b + j * 4096); src[j] += kadv; }
    };
    auto stageB = [&](int slot) {
        LAS char* b = lds + G2_BOFF + slot * G2_BB + w4 * 1024;
#pragma unroll
        for (int j = 0; j < 4; ++j) { glds16(src[j], b + j * 4096); src[j] += kadv; }
    };
    const int fr = lane & 15, fq = lane >> 4;
    const int aoff0 = (w4 * 64 + fr) * 128 + ((fq ^ (fr & 7)) << 4);
    const int aoff1 = (w4 * 64 + fr) * 128 + (((4 + fq) ^ (fr & 7)) << 4);
    const int bl = G2_BOFF + (2 * fq * 8) * 128 + (fr >> 2) * 32 + (fr & 3) * 8 + grp * 4 * 128;
    const int sgn = (fq & 1) * 128;
    const int bo0 = bl + sgn, bo1 = bl - sgn;
    f32x4 acc[4][4];
#pragma unroll
    for (int m = 0; m < 4; ++m)
#pragma unroll
        for (int n = 0; n < 4; ++n) acc[m][n] = f32x4{0.f, 0.f, 0.f, 0.f};
    bf16x8 af[2][4], bfr_[2][4];
    auto ldfr = [&](int aslot, int bslot) {
        LAS char* b = lds + bslot * G2_BB;
        LAS char* a = lds + aslot * G2_AB;
#pragma unroll
        for (int ks = 0; ks < 2; ++ks) {
#pragma unroll
            for (int n = 0; n < 4; ++n) {
                LAS char* pb = b + ((n & 1) ? bo1 : bo0) + n * 128 + ks * (8 * 8 * 128);
                bfr_[ks][n] = cat8(trread(pb), trread(pb + 8 * 128));
            }
#pragma unroll
            for (int m = 0; m < 4; ++m) af[ks][m] = *(LAS bf16x8*)(a + (ks ? aoff1 : aoff0) + m * 2048);
        }
    };
    auto mma = [&]() {
        __builtin_amdgcn_s_setprio(1);
#pragma unroll
        for (int ks = 0; ks < 2; ++ks)
#pragma unroll
            for (int n = 0; n < 4; ++n)
#pragma unroll
                for (int m = 0; m < 4; ++m) acc[m][n] = __builtin_amdgcn_mfma_f32_16x16x32_bf16(bfr_[ks][n], af[ks][m], acc[m][n], 0, 0, 0);
        __builtin_amdgcn_s_setprio(0);
    };
    if (grp == 0) { stageA(0); WAITV(0); }
    else { stageB(0); stageB(1); WAITV(4); }
    BAR();
    if (grp == 0) {
        int bs = 0;
        for (int t = 0; t < NT2; ++t) {
            if (t + 1 < NT2) stageA((t + 1) & 1);
            ldfr(t & 1, bs);
            BAR();
            mma();
            WAITV(0);
            BAR();
            bs = bs == 2 ? 0 : bs + 1;
        }
    } else {
        BAR();
        int bs = 0;
        for (int t = 0; t < NT2; ++t) {
            const int b2 = bs == 0 ? 2 : bs - 1;
            if (t + 2 < NT2) stageB(b2);
            ldfr(t & 1, bs);
            if (t + 2 < NT2) { WAITV(4); } else { WAITV(0); }
            BAR();
            mma();
            if (t + 1 < NT2) BAR();
            bs = bs == 2 ? 0 : bs + 1;
        }
    }
    const int tile = mt * 8 + nt;
    if (w4 != ksp) {
        __amdgpu_buffer_rsrc_t rs = __builtin_amdgcn_make_buffer_rsrc((void*)part, 0, 0x7fffffff, 0x00020000);
        const int off = ((((tile * 4 + w4) * 4 + ksp) * 2 + grp) * 16 * 64 + lane) * 16;
#pragma unroll
        for (int m = 0; m < 4; ++m)
#pragma unroll
            for (int n = 0; n < 4; ++n)
                __builtin_amdgcn_raw_buffer_store_b128(__builtin_bit_cast(u32x4, acc[m][n]), rs, off + (m * 4 + n) * 1024, 0, 16);
    }
    WAITV(0);
    __syncthreads();
    GAS u32* fl = (GAS u32*)flags;
    if (tid == 0) __hip_atomic_store(fl + (tile * 4 + ksp) * 16, 1u, __ATOMIC_RELAXED, __HIP_MEMORY_SCOPE_AGENT);
    if (w4 != ksp) return;
    {
#pragma unroll
        for (int o = 1; o < 4; ++o) {
            const int ks2 = (ksp + o) & 3;
            unsigned spins = 0;
            while (__builtin_amdgcn_readfirstlane(__hip_atomic_load(fl + (tile * 4 + ks2) * 16, __ATOMIC_RELAXED, __HIP_MEMORY_SCOPE_AGENT)) == 0u) {
                __builtin_amdgcn_s_sleep(1);
                if (++spins > (1u << 22)) break;
            }
        }
        __builtin_amdgcn_fence(__ATOMIC_ACQUIRE, "agent");
        WAITV(0);
        const f32x4* pb = part + (size_t)((tile * 4 + w4) * 4 * 2 + grp) * 16 * 64 + lane;
        const f32x4* p1 = pb + (size_t)((ksp + 1) & 3) * 2 * 16 * 64;
        const f32x4* p2 = pb + (size_t)((ksp + 2) & 3) * 2 * 16 * 64;
        const f32x4* p3 = pb + (size_t)((ksp + 3) & 3) * 2 * 16 * 64;
#pragma unroll
        for (int m = 0; m < 4; ++m) {
            const size_t ro = (size_t)(row0 + w4 * 64 + m * 16 + fr) * D_ + col0 + grp * 64 + fq * 4;
            u32x2 hv[4];
#pragma unroll
            for (int n = 0; n < 4; ++n) hv[n] = *(const u32x2*)(h + ro + n * 16);
#pragma unroll
            for (int n = 0; n < 4; ++n) {
                const f32x4 sum = (acc[m][n] + p1[(m * 4 + n) * 64]) + (p2[(m * 4 + n) * 64] + p3[(m * 4 + n) * 64]);
                float r[4];
#pragma unroll
                for (int j = 0; j < 4; ++j) {
                    const u32 hw = hv[n][j >> 1];
                    r[j] = bf2f((j & 1) ? (hw >> 16) : (hw & 0xffffu)) + bfr(sum[j]);
                }
                *(u32x2*)(out + ro + n * 16) = u32x2{pack2(r[0], r[1]), pack2(r[2], r[3])};
            }
        }
    }
}

extern "C" void kernel_launch(void* const* d_in, const int* in_sizes, int n_in, void* d_out, int out_size, void* d_ws, size_t ws_size,
                              hipStream_t stream) {
    const u16* x = (const u16*)d_in[0];
    const u16* wr = (const u16*)d_in[1];
    const u16* wi = (const u16*)d_in[2];
    const u16* wg = (const u16*)d_in[4];
    const u16* wu = (const u16*)d_in[5];
    const u16* wd = (const u16*)d_in[6];
    u16* out = (u16*)d_out;
    char* ws = (char*)d_ws;
    f2* P = (f2*)(ws);
    u32* Wt = (u32*)(ws + (8u << 20));
    u16* h = (u16*)(ws + (13u << 20));
    u16* ln2 = (u16*)(ws + (17u << 20));
    u16* act = (u16*)(ws + (21u << 20));
    f32x4* part = (f32x4*)(ws + (36u << 20));
    u32* flags = (u32*)(ws + (70u << 20));
    k1_ln_fft<<<256 + 288, 512, 0, stream>>>(x, P, wr, wi, Wt, flags);
    k2_col<<<256, 256, 0, stream>>>(P, Wt);
    k3_ifft_ln<<<256, 512, 0, stream>>>(P, x, h, ln2);
    k4_gemm1<<<256, 512, 0, stream>>>(ln2, wg, wu, act);
    k5_gemm2<<<256, 512, 0, stream>>>(act, wd, h, out, part, flags);
}
```

```cpp
#include <hip/hip_runtime.h>
#include <hip/hip_bf16.h>

typedef unsigned short u16;
typedef unsigned int u32;
typedef float f2 __attribute__((ext_vector_type(2)));
typedef float f32x4 __attribute__((ext_vector_type(4)));
typedef short bf16x8 __attribute__((ext_vector_type(8)));
typedef short s16x4 __attribute__((ext_vector_type(4)));
typedef unsigned u32x4 __attribute__((ext_vector_type(4)));
typedef unsigned u32x2 __attribute__((ext_vector_type(2)));
#define LAS __attribute__((address_space(3)))
#define GAS __attribute__((address_space(1)))

constexpr int S_ = 2048, D_ = 1024, DF = 513, FF = 3584;

__device__ __forceinline__ float bf2f(u32 h) { return __uint_as_float(h << 16); }
__device__ __forceinline__ u32 f2bf(float f) { __bf16 b = (__bf16)f; return (u32)__builtin_bit_cast(u16, b); }
__device__ __forceinline__ float bfr(float f) { return bf2f(f2bf(f)); }
typedef __bf16 bf16x2_t __attribute__((ext_vector_type(2)));
__device__ __forceinline__ u32 pack2(float lo, float hi) { return __builtin_bit_cast(u32, __builtin_convertvector(f2{lo, hi}, bf16x2_t)); }

__device__ __forceinline__ f2 cmul(f2 a, f2 b) { return f2{a.x * b.x - a.y * b.y, a.x * b.y + a.y * b.x}; }
__device__ __forceinline__ f2 cmulc(f2 a, f2 b) { return f2{a.x * b.x + a.y * b.y, a.y * b.x - a.x * b.y}; }
__device__ __forceinline__ f2 cconj(f2 a) { return f2{a.x, -a.y}; }
template <int D> __device__ __forceinline__ f2 muli(f2 a) { return D > 0 ? f2{-a.y, a.x} : f2{a.y, -a.x}; }
template <int D> __device__ __forceinline__ f2 twm(f2 a, f2 w) { return D > 0 ? cmulc(a, w) : cmul(a, w); }

template <int D> __device__ __forceinline__ void dft4(f2& x0, f2& x1, f2& x2, f2& x3) {
    f2 t0 = x0 + x2, t1 = x0 - x2, t2 = x1 + x3, t3 = muli<D>(x1 - x3);
    x0 = t0 + t2; x2 = t0 - t2; x1 = t1 + t3; x3 = t1 - t3;
}
template <int D> __device__ __forceinline__ void dft8(f2* x) {
    dft4<D>(x[0], x[2], x[4], x[6]);
    dft4<D>(x[1], x[3], x[5], x[7]);
    const float c = 0.70710678118654752440f;
    f2 o1 = D > 0 ? f2{(x[3].x - x[3].y) * c, (x[3].x + x[3].y) * c} : f2{(x[3].x + x[3].y) * c, (x[3].y - x[3].x) * c};
    f2 o2 = muli<D>(x[5]);
    f2 o3 = D > 0 ? f2{(-x[7].x - x[7].y) * c, (x[7].x - x[7].y) * c} : f2{(x[7].y - x[7].x) * c, (-x[7].x - x[7].y) * c};
    f2 o0 = x[1];
    f2 e0 = x[0], e1 = x[2], e2 = x[4], e3 = x[6];
    x[0] = e0 + o0; x[4] = e0 - o0;
    x[1] = e1 + o1; x[5] = e1 - o1;
    x[2] = e2 + o2; x[6] = e2 - o2;
    x[3] = e3 + o3; x[7] = e3 - o3;
}
template <int D> __device__ __forceinline__ void dft16(f2* x) {
    f2 e[8], o[8];
#pragma unroll
    for (int i = 0; i < 8; ++i) { e[i] = x[2 * i]; o[i] = x[2 * i + 1]; }
    dft8<D>(e); dft8<D>(o);
    const float c1 = 0.92387953251128675613f, s1 = 0.38268343236508977173f, c2 = 0.70710678118654752440f;
    o[1] = twm<D>(o[1], f2{c1, -s1});
    o[2] = twm<D>(o[2], f2{c2, -c2});
    o[3] = twm<D>(o[3], f2{s1, -c1});
    o[4] = muli<D>(o[4]);
    o[5] = twm<D>(o[5], f2{-s1, -c1});
    o[6] = twm<D>(o[6], f2{-c2, -c2});
    o[7] = twm<D>(o[7], f2{-c1, -s1});
#pragma unroll
    for (int i = 0; i < 8; ++i) { x[i] = e[i] + o[i]; x[i + 8] = e[i] - o[i]; }
}
template <int N> __device__ __forceinline__ void twpow(f2* w, int num, float inv_den2) {
    float s, c; sincospif(-(float)num * inv_den2, &s, &c);
    w[1] = f2{c, s};
#pragma unroll
    for (int k = 2; k < N; ++k) w[k] = (k & 1) ? cmul(w[k - 1], w[1]) : cmul(w[k / 2], w[k / 2]);
}
__device__ __forceinline__ float wave_sum(float v) {
#pragma unroll
    for (int o = 32; o >= 1; o >>= 1) v += __shfl_xor(v, o);
    return v;
}
__device__ __forceinline__ void ln_row(float* xe, float* xo) {
    float s = 0.f;
#pragma unroll
    for (int m = 0; m < 8; ++m) s += xe[m] + xo[m];
    s = wave_sum(s);
    const float mean = s * (1.0f / 1024.0f);
    float q = 0.f;
#pragma unroll
    for (int m = 0; m < 8; ++m) { float a = xe[m] - mean, b = xo[m] - mean; q += a * a + b * b; }
    q = wave_sum(q);
    const float mb = bfr(mean), vb = bfr(q * (1.0f / 1024.0f));
    const float den = bfr(sqrtf(bfr(vb + 1e-5f)));
#pragma unroll
    for (int m = 0; m < 8; ++m) { xe[m] = bfr(bfr(xe[m] - mb) / den); xo[m] = bfr(bfr(xo[m] - mb) / den); }
}

constexpr int RS = 580;
__global__ __launch_bounds__(512) void k1_ln_fft(const u16* __restrict__ x, f2* __restrict__ P, const u16* __restrict__ wr,
                                                 const u16* __restrict__ wi, u32* __restrict__ Wt, u32* __restrict__ flags) {
    __shared__ __attribute__((aligned(16))) f2 sm[8 * RS];
    const int tid = threadIdx.x, lane = tid & 63, wv = tid >> 6;
    if (blockIdx.x >= 256) {
        const int b = blockIdx.x - 256;
        if (b == 0 && tid < 256) flags[tid * 16] = 0u;
        u32* tile = (u32*)sm;
        const int k0 = (b & 31) * 64, c0 = (b >> 5) * 64;
#pragma unroll
        for (int i = 0; i < 8; ++i) {
            const int r = wv + 8 * i, c = c0 + lane;
            u32 v = 0;
            if (c < DF) v = (u32)wr[(k0 + r) * DF + c] | ((u32)wi[(k0 + r) * DF + c] << 16);
            tile[r * 65 + lane] = v;
        }
        __syncthreads();
#pragma unroll
        for (int i = 0; i < 8; ++i) {
            const int c = wv + 8 * i;
            if (c0 + c < DF) Wt[(c0 + c) * S_ + k0 + lane] = tile[lane * 65 + c];
        }
        return;
    }
    const int s = blockIdx.x * 8 + wv;
    const u32* xr = (const u32*)x + (size_t)s * 512;
    float xe[8], xo[8];
#pragma unroll
    for (int m = 0; m < 8; ++m) { u32 r = xr[lane + 64 * m]; xe[m] = bf2f(r & 0xffffu); xo[m] = bf2f(r >> 16); }
    ln_row(xe, xo);
    f2 v[8], w[8];
#pragma unroll
    for (int m = 0; m < 8; ++m) v[m] = f2{xe[m], xo[m]};
    f2* R = sm + wv * RS;
    dft8<-1>(v);
    twpow<8>(w, lane, 2.0f / 512.0f);
    R[lane] = v[0];
#pragma unroll
    for (int k = 1; k < 8; ++k) R[k * 72 + lane] = cmul(v[k], w[k]);
    __syncthreads();
    {
        const int k1 = lane >> 3, b = lane & 7;
#pragma unroll
        for (int a = 0; a < 8; ++a) v[a] = R[k1 * 72 + 8 * a + b];
        dft8<-1>(v);
        twpow<8>(w, b, 2.0f / 64.0f);
        __syncthreads();
        R[k1 * 72 + b * 9] = v[0];
#pragma unroll
        for (int k = 1; k < 8; ++k) R[k1 * 72 + b * 9 + k] = cmul(v[k], w[k]);
    }
    __syncthreads();
    {
        const int k1 = lane & 7, k2 = lane >> 3;
#pragma unroll
        for (int b = 0; b < 8; ++b) v[b] = R[k1 * 72 + 9 * b + k2];
        dft8<-1>(v);
    }
    __syncthreads();
#pragma unroll
    for (int k = 0; k < 8; ++k) R[lane + 64 * k] = v[k];
    __syncthreads();
    const int s0 = blockIdx.x * 8;
#pragma unroll
    for (int i = 0; i < 4; ++i) {
        const int it = tid + 512 * i, r = it & 7, p = it >> 3;
        const int ca = p, cb = p ? 512 - p : 256;
        f2 a = sm[r * RS + ca], b = sm[r * RS + cb];
        *(f32x4*)(P + ((size_t)p * S_ + s0 + r) * 2) = f32x4{a.x, a.y, b.x, b.y};
    }
}

constexpr int CS = 2192;
__global__ __launch_bounds__(256) void k2_col(f2* __restrict__ P, const u32* __restrict__ Wt) {
    __shared__ __attribute__((aligned(16))) f2 sm[2 * CS];
    const int p = blockIdx.x, tid = threadIdx.x;
    const int c = tid & 1, t = tid >> 1, b = t & 7, k1 = t >> 3;
    f2* base = P + (size_t)p * 4096;
    f2 v[16], w[16];
#pragma unroll
    for (int m = 0; m < 16; ++m) v[m] = base[(128 * m + t) * 2 + c];
    const int T = tid, Tm = (256 - T) & 255;
    const int colA = p, colB = p ? 512 - p : 256;
    u32 wa[8], wb[8];
    if (p) {
#pragma unroll
        for (int j = 0; j < 8; ++j) { wa[j] = Wt[colA * S_ + T + 256 * j]; wb[j] = Wt[colB * S_ + Tm + 256 * j]; }
    }
    dft16<-1>(v);
    twpow<16>(w, t, 2.0f / 2048.0f);
    f2* C = sm + c * CS;
    C[t] = v[0];
#pragma unroll
    for (int k = 1; k < 16; ++k) C[k * 136 + t] = cmul(v[k], w[k]);
    __syncthreads();
#pragma unroll
    for (int a = 0; a < 16; ++a) v[a] = C[k1 * 136 + 8 * a + b];
    dft16<-1>(v);
    twpow<16>(w, b, 2.0f / 128.0f);
    __syncthreads();
    C[b * 258 + k1] = v[0];
#pragma unroll
    for (int k = 1; k < 16; ++k) C[b * 258 + k1 + 16 * k] = cmul(v[k], w[k]);
    __syncthreads();
    f2* A = v; f2* B = v + 8;
#pragma unroll
    for (int j = 0; j < 8; ++j) { A[j] = sm[j * 258 + T]; B[j] = sm[CS + j * 258 + Tm]; }
    dft8<-1>(A); dft8<-1>(B);
    const float NRM = 1.0f / 4194304.0f;
    if (p) {
        float sn, cn; sincospif(-(float)p * (2.0f / 1024.0f), &sn, &cn);
        const f2 tw = f2{cn, sn};
        if (T == 0) {
            f2 t0 = B[0]; u32 w0 = wb[0];
#pragma unroll
            for (int j = 0; j < 7; ++j) { B[j] = B[j + 1]; wb[j] = wb[j + 1]; }
            B[7] = t0; wb[7] = w0;
        }
#pragma unroll
        for (int j = 0; j < 8; ++j) {
            const f2 za = A[j], zb = B[7 - j];
            const f2 E = f2{za.x + zb.x, za.y - zb.y};
            const f2 Dd = f2{za.x - zb.x, za.y + zb.y};
            const f2 O = f2{Dd.y, -Dd.x};
            const f2 wO = cmul(O, tw);
            const f2 Xa = E + wO, Xb = cconj(E - wO);
            const f2 fa = f2{bf2f(wa[j] & 0xffffu) * NRM, bf2f(wa[j] >> 16) * NRM};
            const f2 fb = f2{bf2f(wb[7 - j] & 0xffffu) * NRM, bf2f(wb[7 - j] >> 16) * NRM};
            const f2 Ya = cmul(Xa, fa), Yb = cmul(Xb, fb);
            const f2 E2 = f2{Ya.x + Yb.x, Ya.y - Yb.y};
            const f2 O2 = cmulc(f2{Ya.x - Yb.x, Ya.y + Yb.y}, tw);
            A[j] = f2{E2.x - O2.y, E2.y + O2.x};
            B[7 - j] = f2{E2.x + O2.y, O2.x - E2.y};
        }
        if (T == 0) {
            f2 t7 = B[7];
#pragma unroll
            for (int j = 7; j > 0; --j) B[j] = B[j - 1];
            B[0] = t7;
        }
    } else {
        __syncthreads();
#pragma unroll
        for (int j = 0; j < 8; ++j) { sm[T + 256 * j] = A[j]; sm[CS + Tm + 256 * j] = B[j]; }
        __syncthreads();
#pragma unroll
        for (int j = 0; j < 8; ++j) {
            {
                const int k = T + 256 * j, km = (2048 - k) & 2047;
                const f2 za = sm[k], zb = sm[km];
                const f2 E = f2{za.x + zb.x, za.y - zb.y};
                const f2 Dd = f2{za.x - zb.x, za.y + zb.y};
                const f2 O = f2{Dd.y, -Dd.x};
                const f2 X0 = E + O, X5 = E - O;
                const u32 u0 = Wt[k], u0m = Wt[km], u5 = Wt[512 * S_ + k], u5m = Wt[512 * S_ + km];
                const f2 y0 = cmul(X0, f2{bf2f(u0 & 0xffffu), bf2f(u0 >> 16)}), y0m = cmul(cconj(X0), f2{bf2f(u0m & 0xffffu), bf2f(u0m >> 16)});
                const f2 y5 = cmul(X5, f2{bf2f(u5 & 0xffffu), bf2f(u5 >> 16)}), y5m = cmul(cconj(X5), f2{bf2f(u5m & 0xffffu), bf2f(u5m >> 16)});
                const f2 s0 = f2{(y0.x + y0m.x) * (0.5f * NRM), (y0.y - y0m.y) * (0.5f * NRM)};
                const f2 s5 = f2{(y5.x + y5m.x) * (0.5f * NRM), (y5.y - y5m.y) * (0.5f * NRM)};
                const f2 E2 = s0 + s5, O2 = s0 - s5;
                A[j] = f2{E2.x - O2.y, E2.y + O2.x};
            }
            {
                const int k = Tm + 256 * j, km = (2048 - k) & 2047;
                const f2 za = sm[CS + k], zb = sm[CS + km];
                const f2 E = f2{za.x + zb.x, za.y - zb.y};
                const f2 Dd = f2{za.x - zb.x, za.y + zb.y};
                const f2 O = f2{Dd.y, -Dd.x};
                const f2 tw = f2{0.f, -1.f};
                const f2 wO = cmul(O, tw);
                const f2 Xa = E + wO, Xb = cconj(E - wO);
                const u32 ua = Wt[256 * S_ + k], ub = Wt[256 * S_ + km];
                const f2 Ya = cmul(Xa, f2{bf2f(ua & 0xffffu) * NRM, bf2f(ua >> 16) * NRM});
                const f2 Yb = cmul(Xb, f2{bf2f(ub & 0xffffu) * NRM, bf2f(ub >> 16) * NRM});
                const f2 E2 = f2{Ya.x + Yb.x, Ya.y - Yb.y};
                const f2 O2 = cmulc(f2{Ya.x - Yb.x, Ya.y + Yb.y}, tw);
                B[j] = f2{E2.x - O2.y, E2.y + O2.x};
            }
        }
    }
    dft8<1>(A); dft8<1>(B);
    __syncthreads();
    {
        twpow<8>(w, T >> 4, 2.0f / 128.0f);
        sm[T] = A[0];
#pragma unroll
        for (int j = 1; j < 8; ++j) sm[j * 258 + T] = cmulc(A[j], w[j]);
        twpow<8>(w, Tm >> 4, 2.0f / 128.0f);
        sm[CS + Tm] = B[0];
#pragma unroll
        for (int j = 1; j < 8; ++j) sm[CS + j * 258 + Tm] = cmulc(B[j], w[j]);
    }
    __syncthreads();
#pragma unroll
    for (int k = 0; k < 16; ++k) v[k] = C[b * 258 + k1 + 16 * k];
    dft16<1>(v);
    __syncthreads();
    {
        float sn, cn;
        sincospif(-(float)(b * k1) * (2.0f / 2048.0f), &sn, &cn);
        f2 wc = f2{cn, sn};
        sincospif(-(float)(8 * k1) * (2.0f / 2048.0f), &sn, &cn);
        const f2 ws = f2{cn, sn};
        w[1] = ws;
#pragma unroll
        for (int k = 2; k < 16; ++k) w[k] = (k & 1) ? cmul(w[k - 1], w[1]) : cmul(w[k / 2], w[k / 2]);
        C[k1 * 136 + b] = cmulc(v[0], wc);
#pragma unroll
        for (int a = 1; a < 16; ++a) C[k1 * 136 + 8 * a + b] = cmulc(v[a], cmul(wc, w[a]));
    }
    __syncthreads();
#pragma unroll
    for (int k = 0; k < 16; ++k) v[k] = C[k * 136 + t];
    dft16<1>(v);
#pragma unroll
    for (int m = 0; m < 16; ++m) base[(128 * m + t) * 2 + c] = v[m];
}

__global__ __launch_bounds__(512) void k3_ifft_ln(const f2* __restrict__ P, const u16* __restrict__ x, u16* __restrict__ h, u16* __restrict__ ln2) {
    __shared__ __attribute__((aligned(16))) f2 sm[8 * RS];
    const int tid = threadIdx.x, lane = tid & 63, wv = tid >> 6;
    const int s0 = blockIdx.x * 8, s = s0 + wv;
    const u32* xr = (const u32*)x + (size_t)s * 512;
    u32 xraw[8];
#pragma unroll
    for (int m = 0; m < 8; ++m) xraw[m] = xr[lane + 64 * m];
#pragma unroll
    for (int i = 0; i < 4; ++i) {
        const int it = tid + 512 * i, r = it & 7, p = it >> 3;
        const int ca = p, cb = p ? 512 - p : 256;
        f32x4 q = *(const f32x4*)(P + ((size_t)p * S_ + s0 + r) * 2);
        sm[r * RS + ca] = f2{q[0], q[1]};
        sm[r * RS + cb] = f2{q[2], q[3]};
    }
    __syncthreads();
    f2* R = sm + wv * RS;
    f2 v[8], w[8];
#pragma unroll
    for (int k = 0; k < 8; ++k) v[k] = R[lane + 64 * k];
    dft8<1>(v);
    __syncthreads();
    {
        const int k1 = lane & 7, k2 = lane >> 3;
        twpow<8>(w, k2, 2.0f / 64.0f);
        R[k1 * 72 + k2] = v[0];
#pragma unroll
        for (int b = 1; b < 8; ++b) R[k1 * 72 + 9 * b + k2] = cmulc(v[b], w[b]);
    }
    __syncthreads();
    {
        const int k1 = lane >> 3, b = lane & 7;
#pragma unroll
        for (int k = 0; k < 8; ++k) v[k] = R[k1 * 72 + b * 9 + k];
        dft8<1>(v);
        __syncthreads();
        float sn, cn;
        sincospif(-(float)(b * k1) * (2.0f / 512.0f), &sn, &cn);
        const f2 wc = f2{cn, sn};
        sincospif(-(float)(8 * k1) * (2.0f / 512.0f), &sn, &cn);
        w[1] = f2{cn, sn};
#pragma unroll
        for (int k = 2; k < 8; ++k) w[k] = (k & 1) ? cmul(w[k - 1], w[1]) : cmul(w[k / 2], w[k / 2]);
        R[k1 * 72 + b] = cmulc(v[0], wc);
#pragma unroll
        for (int a = 1; a < 8; ++a) R[k1 * 72 + 8 * a + b] = cmulc(v[a], cmul(wc, w[a]));
    }
    __syncthreads();
#pragma unroll
    for (int k = 0; k < 8; ++k) v[k] = R[k * 72 + lane];
    dft8<1>(v);
    float he[8], ho[8];
    u32* hr = (u32*)h + (size_t)s * 512;
    u32* lr = (u32*)ln2 + (size_t)s * 512;
#pragma unroll
    for (int m = 0; m < 8; ++m) {
        he[m] = bfr(bf2f(xraw[m] & 0xffffu) + bfr(v[m].x));
        ho[m] = bfr(bf2f(xraw[m] >> 16) + bfr(v[m].y));
        hr[lane + 64 * m] = f2bf(he[m]) | (f2bf(ho[m]) << 16);
    }
    ln_row(he, ho);
#pragma unroll
    for (int m = 0; m < 8; ++m) lr[lane + 64 * m] = f2bf(he[m]) | (f2bf(ho[m]) << 16);
}

__device__ __forceinline__ void glds16(const void* g, LAS void* l) {
    unsigned sv; const unsigned lb = (unsigned)__builtin_amdgcn_readfirstlane((unsigned)(size_t)l);
    asm volatile("s_mov_b32 %0, m0\n\ts_mov_b32 m0, %2\n\ts_nop 0\n\tglobal_load_lds_dwordx4 %1, off\n\ts_mov_b32 m0, %0" : "=&s"(sv) : "v"(g), "s"(lb) : "memory");
}
typedef short v4i16_t __attribute__((ext_vector_type(4)));
__device__ __forceinline__ s16x4 trread(LAS char* p) { return __builtin_bit_cast(s16x4, __builtin_amdgcn_ds_read_tr16_b64_v4i16((LAS v4i16_t*)p)); }
__device__ __forceinline__ bf16x8 cat8(s16x4 a, s16x4 b) { return bf16x8{a[0], a[1], a[2], a[3], b[0], b[1], b[2], b[3]}; }
#define WAITV0() asm volatile("s_waitcnt vmcnt(0)" ::: "memory")

__device__ __forceinline__ float gelu_tanh(float x) {
    const float k0 = 0.7978845608028654f, k1 = 0.044715f;
    const float u = k0 * (x + k1 * x * x * x);
    const float e = __expf(-2.0f * u);
    return x / (1.0f + e);
}

__device__ __forceinline__ f2 geglu2(f2 g, f2 u) {
    const float c0 = -2.0f * 0.7978845608028654f * 1.4426950408889634f, c1 = c0 * 0.044715f;
    const f2 z = g * g;
    const f2 t = g * (z * c1 + c0);
    f2 e; e.x = __builtin_amdgcn_exp2f(t.x); e.y = __builtin_amdgcn_exp2f(t.y);
    const f2 d = e + 1.0f;
    f2 r; r.x = __builtin_amdgcn_rcpf(d.x); r.y = __builtin_amdgcn_rcpf(d.y);
    return (g * r) * u;
}
__device__ __forceinline__ f32x4 geglu4(f32x4 g, f32x4 u) {
    const f2 a = geglu2(f2{g[0], g[1]}, f2{u[0], u[1]}), b = geglu2(f2{g[2], g[3]}, f2{u[2], u[3]});
    return f32x4{a.x, a.y, b.x, b.y};
}

#define BAR() do { asm volatile("s_waitcnt lgkmcnt(0)" ::: "memory"); __builtin_amdgcn_s_barrier(); __builtin_amdgcn_sched_barrier(0); } while (0)
#define WAITV(n) asm volatile("s_waitcnt vmcnt(" #n ")" ::: "memory")
constexpr int G1_AB = 32768, G1_BB = 28672, G1_BOFF = 2 * G1_AB, NT1 = D_ / 64;
__global__ __launch_bounds__(512, 2) void k4_gemm1(const u16* __restrict__ A, const u16* __restrict__ Wg, const u16* __restrict__ Wu, u16* __restrict__ act) {
    __shared__ __attribute__((aligned(16))) char smem[2 * G1_AB + 3 * G1_BB];
    LAS char* lds = (LAS char*)smem;
    const int tid = threadIdx.x, lane = tid & 63, wv = __builtin_amdgcn_readfirstlane(tid >> 6);
    const int bid = blockIdx.x, xcd = bid & 7, loc = bid >> 3;
    const int mt = (xcd & 1) * 4 + (loc & 3), nt = (xcd >> 1) * 8 + (loc >> 2);
    const int row0 = mt * 256, col0 = nt * 112;
    const int grp = wv >> 2, w4 = wv & 3;
    const char* src[8];
    size_t kadv;
    if (grp == 0) {
#pragma unroll
        for (int j = 0; j < 8; ++j) {
            const int pc = w4 + 4 * j, r = pc * 8 + (lane >> 3), cp = lane & 7, cc = cp ^ (r & 7);
            src[j] = (const char*)(A + (size_t)(row0 + r) * D_ + cc * 8);
        }
        kadv = 128;
    } else {
#pragma unroll
        for (int j = 0; j < 8; ++j) {
            int pc = w4 + 4 * j; if (pc >= 28) pc = 0;
            const int ch = pc * 64 + lane, blk = ch >> 3, wi = ch & 7, q = wi >> 1, hf = wi & 1;
            const int kb = blk / 14, pos = blk % 14, nb = pos ^ ((kb >> 1) & 1), k = 4 * kb + q;
            src[j] = nb < 7 ? (const char*)(Wg + (size_t)k * FF + col0 + nb * 16 + hf * 8)
                            : (const char*)(Wu + (size_t)k * FF + col0 + (nb - 7) * 16 + (hf ^ 1) * 8);
        }
        kadv = (size_t)64 * FF * 2;
    }
    auto stageA = [&](int slot) {
        LAS char* b = lds + slot * G1_AB + w4 * 1024;
#pragma unroll
        for (int j = 0; j < 8; ++j) { glds16(src[j], b + j * 4096); src[j] += kadv; }
    };
    auto stageB = [&](int slot) {
        LAS char* b = lds + G1_BOFF + slot * G1_BB + w4 * 1024;
#pragma unroll
        for (int j = 0; j < 7; ++j) { glds16(src[j], b + j * 4096); src[j] += kadv; }
    };
    const int fr = lane & 15, fq = lane >> 4;
    const int aoff0 = (w4 * 64 + fr) * 128 + ((fq ^ (fr & 7)) << 4);
    const int aoff1 = (w4 * 64 + fr) * 128 + (((4 + fq) ^ (fr & 7)) << 4);
    int bo[7];
#pragma unroll
    for (int n = 0; n < 7; ++n) {
        const int j = grp * 7 + n, isup = (fr >> 1) & 1, blk = isup * 7 + (j >> 1);
        bo[n] = G1_BOFF + (2 * fq * 14 + (blk ^ (fq & 1))) * 128 + (fr >> 2) * 32 + (((j & 1) ^ isup) << 4) + (fr & 1) * 8;
    }
    f32x4 acc[4][7];
#pragma unroll
    for (int m = 0; m < 4; ++m)
#pragma unroll
        for (int n = 0; n < 7; ++n) acc[m][n] = f32x4{0.f, 0.f, 0.f, 0.f};
    bf16x8 af[2][4], bfr_[2][7];
    auto ldfr = [&](int aslot, int bslot) {
        LAS char* b = lds + bslot * G1_BB;
        LAS char* a = lds + aslot * G1_AB;
#pragma unroll
        for (int ks = 0; ks < 2; ++ks) {
#pragma unroll
            for (int n = 0; n < 7; ++n) {
                LAS char* pb = b + bo[n] + ks * (8 * 14 * 128);
                bfr_[ks][n] = cat8(trread(pb), trread(pb + 14 * 128));
            }
#pragma unroll
            for (int m = 0; m < 4; ++m) af[ks][m] = *(LAS bf16x8*)(a + (ks ? aoff1 : aoff0) + m * 2048);
        }
    };
    auto mma = [&]() {
        __builtin_amdgcn_s_setprio(1);
#pragma unroll
        for (int ks = 0; ks < 2; ++ks)
#pragma unroll
            for (int n = 0; n < 7; ++n)
#pragma unroll
                for (int m = 0; m < 4; ++m) acc[m][n] = __builtin_amdgcn_mfma_f32_16x16x32_bf16(bfr_[ks][n], af[ks][m], acc[m][n], 0, 0, 0);
        __builtin_amdgcn_s_setprio(0);
    };
    if (grp == 0) { stageA(0); WAITV(0); }
    else { stageB(0); stageB(1); WAITV(7); }
    BAR();
    if (grp == 0) {
        int bs = 0;
        for (int t = 0; t < NT1; ++t) {
            if (t + 1 < NT1) stageA((t + 1) & 1);
            ldfr(t & 1, bs);
            BAR();
            mma();
            WAITV(0);
            BAR();
            bs = bs == 2 ? 0 : bs + 1;
        }
    } else {
        BAR();
        int bs = 0;
        for (int t = 0; t < NT1; ++t) {
            const int b2 = bs == 0 ? 2 : bs - 1;
            if (t + 2 < NT1) stageB(b2);
            ldfr(t & 1, bs);
            if (t + 2 < NT1) { WAITV(7); } else { WAITV(0); }
            BAR();
            mma();
            if (t + 1 < NT1) BAR();
            bs = bs == 2 ? 0 : bs + 1;
        }
    }
    {
        char* ob = (char*)act + ((size_t)((row0 >> 4) + w4 * 4) * (FF / 8) + (col0 >> 3) + grp * 7) * 256 + fr * 16;
        auto grp4 = [&](f32x4& t0, f32x4& t1, f32x4& t2, f32x4& t3, size_t o0, size_t o1, size_t o2, size_t o3) {
            float gp[4], up[4], gq[4], uq[4];
#pragma unroll
            for (int r = 0; r < 4; ++r) {
                auto x = __builtin_amdgcn_permlane32_swap(__float_as_uint(t0[r]), __float_as_uint(t2[r]), false, false);
                gp[r] = __uint_as_float(x[0]); up[r] = __uint_as_float(x[1]);
                auto y = __builtin_amdgcn_permlane32_swap(__float_as_uint(t1[r]), __float_as_uint(t3[r]), false, false);
                gq[r] = __uint_as_float(y[0]); uq[r] = __uint_as_float(y[1]);
            }
            const f2 pa = geglu2(f2{gp[0], gp[1]}, f2{up[0], up[1]}), pb = geglu2(f2{gp[2], gp[3]}, f2{up[2], up[3]});
            const f2 qa = geglu2(f2{gq[0], gq[1]}, f2{uq[0], uq[1]}), qb = geglu2(f2{gq[2], gq[3]}, f2{uq[2], uq[3]});
            const u32 p0 = pack2(pa.x, pa.y), p1 = pack2(pb.x, pb.y), q0 = pack2(qa.x, qa.y), q1 = pack2(qb.x, qb.y);
            auto s0 = __builtin_amdgcn_permlane16_swap(p0, q0, false, false);
            auto s1 = __builtin_amdgcn_permlane16_swap(p1, q1, false, false);
            const size_t o = fq == 0 ? o0 : fq == 1 ? o1 : fq == 2 ? o2 : o3;
            *(u32x4*)(ob + o) = u32x4{s0[0], s1[0], s0[1], s1[1]};
        };
        constexpr size_t MS = (size_t)(FF / 8) * 256;
#pragma unroll
        for (int m = 0; m < 4; ++m)
            grp4(acc[m][0], acc[m][1], acc[m][2], acc[m][3], m * MS, m * MS + 256, m * MS + 512, m * MS + 768);
#pragma unroll
        for (int n = 4; n < 7; ++n)
            grp4(acc[0][n], acc[1][n], acc[2][n], acc[3][n], n * 256, MS + n * 256, 2 * MS + n * 256, 3 * MS + n * 256);
    }
}

constexpr int G2_U = 32768, NU2 = FF / 2 / 64;
__global__ __launch_bounds__(512, 2) void k5_gemm2(const u16* __restrict__ A, const u16* __restrict__ Wd, const u16* __restrict__ h,
                                                   u16* __restrict__ out, f32x4* __restrict__ part, u32* __restrict__ flags) {
    __shared__ __attribute__((aligned(16))) char smem[4 * G2_U];
    LAS char* lds = (LAS char*)smem;
    const int tid = threadIdx.x, lane = tid & 63, wv = __builtin_amdgcn_readfirstlane(tid >> 6);
    const int bid = blockIdx.x, xcd = bid & 7, loc = bid >> 3;
    const int mt = (xcd >> 1) * 4 + (loc & 3), nt = (xcd & 1) * 4 + ((loc >> 2) & 3), ksp = loc >> 4;
    const int row0 = mt * 128, col0 = nt * 128, kbase = ksp * (FF / 2);
    const int grp = wv >> 2, w4 = wv & 3;
    const char* src[8];
#pragma unroll
    for (int j = 0; j < 4; ++j) {
        const int pc = w4 + 4 * j, c = pc >> 1, r = (pc & 1) * 64 + lane;
        src[j] = (const char*)A + ((size_t)((row0 + r) >> 4) * (FF / 8) + (kbase >> 3) + c) * 256 + (r & 15) * 16;
    }
#pragma unroll
    for (int j = 0; j < 4; ++j) {
        const int pc = w4 + 4 * j, ch = pc * 64 + lane, blk = ch >> 3, wi = ch & 7, q = wi >> 1, hf = wi & 1;
        const int kb = blk >> 3, pos = blk & 7, nb = pos ^ ((kb >> 1) & 1), k = 4 * kb + q;
        src[4 + j] = (const char*)(Wd + (size_t)(kbase + k) * D_ + col0 + nb * 16 + hf * 8);
    }
    auto stage = [&](int u) {
        LAS char* b = lds + (u & 3) * G2_U + w4 * 1024;
        const size_t ao = (size_t)u * 8 * 256, bo = (size_t)u * 64 * D_ * 2;
#pragma unroll
        for (int j = 0; j < 4; ++j) glds16(src[j] + ao, b + j * 4096);
#pragma unroll
        for (int j = 0; j < 4; ++j) glds16(src[4 + j] + bo, b + 16384 + j * 4096);
    };
    const int fr = lane & 15, fq = lane >> 4, wr = w4 >> 1, wc = w4 & 1;
    const int aoff = fq * 2048 + (wr * 64 + fr) * 16;
    const int bl = 16384 + (2 * fq * 8) * 128 + (fr >> 2) * 32 + (fr & 3) * 8 + wc * 4 * 128;
    const int sgn = (fq & 1) * 128;
    const int bo0 = bl + sgn, bo1 = bl - sgn;
    f32x4 acc[4][4];
#pragma unroll
    for (int m = 0; m < 4; ++m)
#pragma unroll
        for (int n = 0; n < 4; ++n) acc[m][n] = f32x4{0.f, 0.f, 0.f, 0.f};
    bf16x8 af[2][4], bfr_[2][4];
    auto ldfr = [&](int u) {
        LAS char* b = lds + (u & 3) * G2_U;
#pragma unroll
        for (int ks = 0; ks < 2; ++ks) {
#pragma unroll
            for (int n = 0; n < 4; ++n) {
                LAS char* pb = b + ((n & 1) ? bo1 : bo0) + n * 128 + ks * (8 * 8 * 128);
                bfr_[ks][n] = cat8(trread(pb), trread(pb + 8 * 128));
            }
#pragma unroll
            for (int m = 0; m < 4; ++m) af[ks][m] = *(LAS bf16x8*)(b + aoff + ks * 8192 + m * 256);
        }
    };
    auto mma = [&]() {
        __builtin_amdgcn_s_setprio(1);
#pragma unroll
        for (int ks = 0; ks < 2; ++ks)
#pragma unroll
            for (int n = 0; n < 4; ++n)
#pragma unroll
                for (int m = 0; m < 4; ++m) acc[m][n] = __builtin_amdgcn_mfma_f32_16x16x32_bf16(bfr_[ks][n], af[ks][m], acc[m][n], 0, 0, 0);
        __builtin_amdgcn_s_setprio(0);
    };
    if (grp == 0) { stage(0); stage(2); WAITV(8); }
    else { stage(1); }
    BAR();
    if (grp == 0) {
        for (int u = 0; u < NU2; u += 2) {
            if (u + 3 < NU2) stage(u + 3);
            ldfr(u);
            if (u + 3 < NU2) { WAITV(8); } else { WAITV(0); }
            BAR();
            mma();
            BAR();
        }
    } else {
        WAITV(0);
        BAR();
        for (int u = 1; u < NU2; u += 2) {
            if (u + 3 < NU2) stage(u + 3);
            ldfr(u);
            if (u + 3 < NU2) { WAITV(8); } else { WAITV(0); }
            BAR();
            mma();
            if (u + 1 < NU2) BAR();
        }
    }
    __syncthreads();
    LAS f32x4* ex = (LAS f32x4*)lds + w4 * 16 * 64 + lane;
    if (grp == 1) {
#pragma unroll
        for (int m = 0; m < 4; ++m)
#pragma unroll
            for (int n = 0; n < 4; ++n) ex[(m * 4 + n) * 64] = acc[m][n];
    }
    __syncthreads();
    const int tile = mt * 8 + nt;
    if (grp == 0) {
#pragma unroll
        for (int m = 0; m < 4; ++m)
#pragma unroll
            for (int n = 0; n < 4; ++n) acc[m][n] += ex[(m * 4 + n) * 64];
        if (wr != ksp) {
            __amdgpu_buffer_rsrc_t rs = __builtin_amdgcn_make_buffer_rsrc((void*)part, 0, 0x7fffffff, 0x00020000);
            const int off = (((tile * 2 + ksp) * 2 + wc) * 16 * 64 + lane) * 16;
#pragma unroll
            for (int m = 0; m < 4; ++m)
#pragma unroll
                for (int n = 0; n < 4; ++n)
                    __builtin_amdgcn_raw_buffer_store_b128(__builtin_bit_cast(u32x4, acc[m][n]), rs, off + (m * 4 + n) * 1024, 0, 16);
            WAITV(0);
        }
    }
    __syncthreads();
    GAS u32* fl = (GAS u32*)flags;
    if (tid == 0) __hip_atomic_store(fl + (tile * 2 + ksp) * 16, 1u, __ATOMIC_RELAXED, __HIP_MEMORY_SCOPE_AGENT);
    if (grp == 0 && wr == ksp) {
        u32x2 hv[4][4];
        const size_t ro = (size_t)(row0 + wr * 64 + fr) * D_ + col0 + wc * 64 + fq * 4;
#pragma unroll
        for (int m = 0; m < 4; ++m)
#pragma unroll
            for (int n = 0; n < 4; ++n) hv[m][n] = *(const u32x2*)(h + ro + (size_t)m * 16 * D_ + n * 16);
        unsigned spins = 0;
        while (__builtin_amdgcn_readfirstlane(__hip_atomic_load(fl + (tile * 2 + (ksp ^ 1)) * 16, __ATOMIC_RELAXED, __HIP_MEMORY_SCOPE_AGENT)) == 0u) {
            __builtin_amdgcn_s_sleep(1);
            if (++spins > (1u << 22)) break;
        }
        __builtin_amdgcn_fence(__ATOMIC_ACQUIRE, "agent");
        WAITV(0);
        const f32x4* theirs = part + (size_t)((tile * 2 + (ksp ^ 1)) * 2 + wc) * 16 * 64 + lane;
        f32x4 o[4][4];
#pragma unroll
        for (int m = 0; m < 4; ++m)
#pragma unroll
            for (int n = 0; n < 4; ++n) o[m][n] = theirs[(m * 4 + n) * 64];
#pragma unroll
        for (int m = 0; m < 4; ++m)
#pragma unroll
            for (int n = 0; n < 4; ++n) {
                const f32x4 sum = acc[m][n] + o[m][n];
                float r[4];
#pragma unroll
                for (int j = 0; j < 4; ++j) {
                    const u32 hw = hv[m][n][j >> 1];
                    r[j] = bf2f((j & 1) ? (hw >> 16) : (hw & 0xffffu)) + bfr(sum[j]);
                }
                *(u32x2*)(out + ro + (size_t)m * 16 * D_ + n * 16) = u32x2{pack2(r[0], r[1]), pack2(r[2], r[3])};
            }
    }
}

extern "C" void kernel_launch(void* const* d_in, const int* in_sizes, int n_in, void* d_out, int out_size, void* d_ws, size_t ws_size,
                              hipStream_t stream) {
    const u16* x = (const u16*)d_in[0];
    const u16* wr = (const u16*)d_in[1];
    const u16* wi = (const u16*)d_in[2];
    const u16* wg = (const u16*)d_in[4];
    const u16* wu = (const u16*)d_in[5];
    const u16* wd = (const u16*)d_in[6];
    u16* out = (u16*)d_out;
    char* ws = (char*)d_ws;
    f2* P = (f2*)(ws);
    u32* Wt = (u32*)(ws + (8u << 20));
    u16* h = (u16*)(ws + (13u << 20));
    u16* ln2 = (u16*)(ws + (17u << 20));
    u16* act = (u16*)(ws + (21u << 20));
    f32x4* part = (f32x4*)(ws + (36u << 20));
    u32* flags = (u32*)(ws + (70u << 20));
    k1_ln_fft<<<256 + 288, 512, 0, stream>>>(x, P, wr, wi, Wt, flags);
    k2_col<<<256, 256, 0, stream>>>(P, Wt);
    k3_ifft_ln<<<256, 512, 0, stream>>>(P, x, h, ln2);
    k4_gemm1<<<256, 512, 0, stream>>>(ln2, wg, wu, act);
    k5_gemm2<<<256, 512, 0, stream>>>(act, wd, h, out, part, flags);
}
```

```cpp
#include <hip/hip_runtime.h>
#include <hip/hip_bf16.h>

typedef unsigned short u16;
typedef unsigned int u32;
typedef float f2 __attribute__((ext_vector_type(2)));
typedef float f32x4 __attribute__((ext_vector_type(4)));
typedef short bf16x8 __attribute__((ext_vector_type(8)));
typedef short s16x4 __attribute__((ext_vector_type(4)));
typedef unsigned u32x4 __attribute__((ext_vector_type(4)));
typedef unsigned u32x2 __attribute__((ext_vector_type(2)));
#define LAS __attribute__((address_space(3)))
#define GAS __attribute__((address_space(1)))

constexpr int S_ = 2048, D_ = 1024, DF = 513, FF = 3584;

__device__ __forceinline__ float bf2f(u32 h) { return __uint_as_float(h << 16); }
__device__ __forceinline__ u32 f2bf(float f) { __bf16 b = (__bf16)f; return (u32)__builtin_bit_cast(u16, b); }
__device__ __forceinline__ float bfr(float f) { return bf2f(f2bf(f)); }
typedef __bf16 bf16x2_t __attribute__((ext_vector_type(2)));
__device__ __forceinline__ u32 pack2(float lo, float hi) { return __builtin_bit_cast(u32, __builtin_convertvector(f2{lo, hi}, bf16x2_t)); }

__device__ __forceinline__ f2 add_ib(f2 a, f2 b) {
    f2 r; asm("v_pk_add_f32 %0, %1, %2 op_sel:[0,1] op_sel_hi:[1,0] neg_lo:[0,1]" : "=v"(r) : "v"(a), "v"(b)); return r; }
__device__ __forceinline__ f2 sub_ib(f2 a, f2 b) {
    f2 r; asm("v_pk_add_f32 %0, %1, %2 op_sel:[0,1] op_sel_hi:[1,0] neg_hi:[0,1]" : "=v"(r) : "v"(a), "v"(b)); return r; }
__device__ __forceinline__ f2 add_cj(f2 a, f2 b) {
    f2 r; asm("v_pk_add_f32 %0, %1, %2 neg_hi:[0,1]" : "=v"(r) : "v"(a), "v"(b)); return r; }
__device__ __forceinline__ f2 sub_cj(f2 a, f2 b) {
    f2 r; asm("v_pk_add_f32 %0, %1, %2 neg_lo:[0,1]" : "=v"(r) : "v"(a), "v"(b)); return r; }
__device__ __forceinline__ f2 cj_sub_ib(f2 a, f2 b) {
    f2 r; asm("v_pk_add_f32 %0, %1, %2 op_sel:[0,1] op_sel_hi:[1,0] neg_hi:[1,0]" : "=v"(r) : "v"(a), "v"(b)); return r; }
__device__ __forceinline__ f2 cmul(f2 a, f2 w) {
    f2 t, r;
    asm("v_pk_mul_f32 %0, %1, %2 op_sel:[0,0] op_sel_hi:[0,1]" : "=v"(t) : "v"(a), "v"(w));
    asm("v_pk_fma_f32 %0, %1, %2, %3 op_sel:[1,1,0] op_sel_hi:[1,0,1] neg_lo:[0,1,0]" : "=v"(r) : "v"(a), "v"(w), "v"(t));
    return r; }
__device__ __forceinline__ f2 cmulc(f2 a, f2 w) {
    f2 t, r;
    asm("v_pk_mul_f32 %0, %1, %2 op_sel:[0,0] op_sel_hi:[0,1] neg_hi:[0,1]" : "=v"(t) : "v"(a), "v"(w));
    asm("v_pk_fma_f32 %0, %1, %2, %3 op_sel:[1,1,0] op_sel_hi:[1,0,1]" : "=v"(r) : "v"(a), "v"(w), "v"(t));
    return r; }
template <int D> __device__ __forceinline__ f2 twm(f2 a, f2 w) { return D > 0 ? cmulc(a, w) : cmul(a, w); }
template <int D> __device__ __forceinline__ f2 add_di(f2 a, f2 b) { return D > 0 ? add_ib(a, b) : sub_ib(a, b); }
template <int D> __device__ __forceinline__ f2 sub_di(f2 a, f2 b) { return D > 0 ? sub_ib(a, b) : add_ib(a, b); }

template <int D> __device__ __forceinline__ void dft4(f2& x0, f2& x1, f2& x2, f2& x3) {
    const f2 t0 = x0 + x2, t1 = x0 - x2, t2 = x1 + x3, d = x1 - x3;
    x0 = t0 + t2; x2 = t0 - t2; x1 = add_di<D>(t1, d); x3 = sub_di<D>(t1, d);
}
template <int D> __device__ __forceinline__ void dft8(f2* x) {
    dft4<D>(x[0], x[2], x[4], x[6]);
    dft4<D>(x[1], x[3], x[5], x[7]);
    const float c = 0.70710678118654752440f;
    const f2 o1 = add_di<D>(x[3], x[3]) * c;
    const f2 o3 = sub_di<D>(x[7], x[7]) * c;
    const f2 e0 = x[0], e1 = x[2], e2 = x[4], e3 = x[6], o0 = x[1], o2 = x[5];
    x[0] = e0 + o0; x[4] = e0 - o0;
    x[1] = e1 + o1; x[5] = e1 - o1;
    x[2] = add_di<D>(e2, o2); x[6] = sub_di<D>(e2, o2);
    x[3] = e3 - o3; x[7] = e3 + o3;
}
template <int D> __device__ __forceinline__ void dft16(f2* x) {
    f2 e[8], o[8];
#pragma unroll
    for (int i = 0; i < 8; ++i) { e[i] = x[2 * i]; o[i] = x[2 * i + 1]; }
    dft8<D>(e); dft8<D>(o);
    const float c1 = 0.92387953251128675613f, s1 = 0.38268343236508977173f, c2 = 0.70710678118654752440f;
    o[1] = twm<D>(o[1], f2{c1, -s1});
    o[2] = add_di<D>(o[2], o[2]) * c2;
    o[3] = twm<D>(o[3], f2{s1, -c1});
    o[5] = twm<D>(o[5], f2{-s1, -c1});
    o[6] = sub_di<D>(o[6], o[6]) * c2;
    o[7] = twm<D>(o[7], f2{-c1, -s1});
#pragma unroll
    for (int i = 0; i < 8; ++i) {
        if (i == 4) { x[4] = add_di<D>(e[4], o[4]); x[12] = sub_di<D>(e[4], o[4]); }
        else if (i == 6) { x[6] = e[6] - o[6]; x[14] = e[6] + o[6]; }
        else { x[i] = e[i] + o[i]; x[i + 8] = e[i] - o[i]; }
    }
}
struct TwTab { float v[4096]; };
constexpr double c_sin(double x) { double t = x, r = x; for (int i = 1; i < 12; ++i) { t *= -x * x / ((2 * i) * (2 * i + 1)); r += t; } return r; }
constexpr double c_cos(double x) { double t = 1, r = 1; for (int i = 1; i < 12; ++i) { t *= -x * x / ((2 * i - 1) * (2 * i)); r += t; } return r; }
constexpr TwTab make_tw() {
    TwTab t{};
    const double step = 6.283185307179586476925286766559 / 2048.0;
    for (int k = 0; k < 2048; ++k) {
        const int q = k >> 9, r = k & 511;
        double c, s;
        if (r <= 256) { c = c_cos(r * step); s = c_sin(r * step); } else { c = c_sin((512 - r) * step); s = c_cos((512 - r) * step); }
        double cc = q == 0 ? c : q == 1 ? -s : q == 2 ? -c : s;
        double ss = q == 0 ? s : q == 1 ? c : q == 2 ? -s : -c;
        t.v[2 * k] = (float)cc; t.v[2 * k + 1] = (float)(-ss);
    }
    return t;
}
__device__ const TwTab TW = make_tw();
__device__ __forceinline__ f2 twl(int k) { return *(const f2*)(TW.v + 2 * k); }
template <int N> __device__ __forceinline__ void twchain(f2* w) {
#pragma unroll
    for (int k = 2; k < N; ++k) w[k] = (k & 1) ? cmul(w[k - 1], w[1]) : cmul(w[k / 2], w[k / 2]);
}
__device__ __forceinline__ float wave_sum(float v) {
#pragma unroll
    for (int o = 32; o >= 1; o >>= 1) v += __shfl_xor(v, o);
    return v;
}
__device__ __forceinline__ void ln_row(f2* x) {
    f2 s2 = (x[0] + x[1]) + (x[2] + x[3]) + ((x[4] + x[5]) + (x[6] + x[7]));
    const float mean = wave_sum(s2.x + s2.y) * (1.0f / 1024.0f);
    f2 q2 = f2{0.f, 0.f};
#pragma unroll
    for (int m = 0; m < 8; ++m) { const f2 d = x[m] - mean; q2 += d * d; }
    const float var = wave_sum(q2.x + q2.y) * (1.0f / 1024.0f);
    const float mb = bfr(mean);
    const float rden = 1.0f / bfr(sqrtf(bfr(bfr(var) + 1e-5f)));
#pragma unroll
    for (int m = 0; m < 8; ++m) {
        const f2 d = x[m] - mb;
        const u32 db = pack2(d.x, d.y);
        const f2 q = f2{bf2f(db & 0xffffu), __uint_as_float(db & 0xffff0000u)} * rden;
        const u32 qb = pack2(q.x, q.y);
        x[m] = f2{bf2f(qb & 0xffffu), __uint_as_float(qb & 0xffff0000u)};
    }
}

constexpr int RS = 580;
__global__ __launch_bounds__(512) void k1_ln_fft(const u16* __restrict__ x, f2* __restrict__ P, const u16* __restrict__ wr,
                                                 const u16* __restrict__ wi, u32* __restrict__ Wt, u32* __restrict__ flags) {
    __shared__ __attribute__((aligned(16))) f2 sm[8 * RS];
    const int tid = threadIdx.x, lane = tid & 63, wv = tid >> 6;
    if (blockIdx.x >= 256) {
        const int b = blockIdx.x - 256;
        if (b == 0 && tid < 256) flags[tid * 16] = 0u;
        u32* tile = (u32*)sm;
        const int k0 = (b & 31) * 64, c0 = (b >> 5) * 64;
#pragma unroll
        for (int i = 0; i < 8; ++i) {
            const int r = wv + 8 * i, c = c0 + lane;
            u32 v = 0;
            if (c < DF) v = (u32)wr[(k0 + r) * DF + c] | ((u32)wi[(k0 + r) * DF + c] << 16);
            tile[r * 65 + lane] = v;
        }
        __syncthreads();
#pragma unroll
        for (int i = 0; i < 8; ++i) {
            const int c = wv + 8 * i;
            if (c0 + c < DF) Wt[(c0 + c) * S_ + k0 + lane] = tile[lane * 65 + c];
        }
        return;
    }
    const int s = blockIdx.x * 8 + wv;
    const u32* xr = (const u32*)x + (size_t)s * 512;
    f2 v[8], w1[8], w2[8];
    u32 raw[8];
#pragma unroll
    for (int m = 0; m < 8; ++m) raw[m] = xr[lane + 64 * m];
    w1[1] = twl(4 * lane); w2[1] = twl(32 * (lane & 7));
#pragma unroll
    for (int m = 0; m < 8; ++m) v[m] = f2{bf2f(raw[m] & 0xffffu), __uint_as_float(raw[m] & 0xffff0000u)};
    ln_row(v);
    f2* R = sm + wv * RS;
    dft8<-1>(v);
    twchain<8>(w1);
    R[lane] = v[0];
#pragma unroll
    for (int k = 1; k < 8; ++k) R[k * 72 + lane] = cmul(v[k], w1[k]);
    __builtin_amdgcn_wave_barrier();
    {
        const int k1 = lane >> 3, b = lane & 7;
#pragma unroll
        for (int a = 0; a < 8; ++a) v[a] = R[k1 * 72 + 8 * a + b];
        dft8<-1>(v);
        twchain<8>(w2);
        __builtin_amdgcn_wave_barrier();
        R[k1 * 72 + b * 9] = v[0];
#pragma unroll
        for (int k = 1; k < 8; ++k) R[k1 * 72 + b * 9 + k] = cmul(v[k], w2[k]);
    }
    __builtin_amdgcn_wave_barrier();
    {
        const int k1 = lane & 7, k2 = lane >> 3;
#pragma unroll
        for (int b = 0; b < 8; ++b) v[b] = R[k1 * 72 + 9 * b + k2];
        dft8<-1>(v);
    }
    __builtin_amdgcn_wave_barrier();
#pragma unroll
    for (int k = 0; k < 8; ++k) R[lane + 64 * k] = v[k];
    __syncthreads();
    const int s0 = blockIdx.x * 8;
#pragma unroll
    for (int i = 0; i < 4; ++i) {
        const int it = tid + 512 * i, r = it & 7, p = it >> 3;
        const int ca = p, cb = p ? 512 - p : 256;
        f2 a = sm[r * RS + ca], b = sm[r * RS + cb];
        *(f32x4*)(P + ((size_t)p * S_ + s0 + r) * 2) = f32x4{a.x, a.y, b.x, b.y};
    }
}

constexpr int CS = 2192;
__device__ __forceinline__ f2 unpk(u32 u) { return f2{bf2f(u & 0xffffu), __uint_as_float(u & 0xffff0000u)}; }
__global__ __launch_bounds__(256) void k2_col(f2* __restrict__ P, const u32* __restrict__ Wt) {
    __shared__ __attribute__((aligned(16))) f2 sm[2 * CS];
    const int p = blockIdx.x, tid = threadIdx.x;
    const int c = tid & 1, t = tid >> 1, b = t & 7, k1 = t >> 3;
    f2* base = P + (size_t)p * 4096;
    f2 v[16], w1[16], w2[16];
#pragma unroll
    for (int m = 0; m < 16; ++m) v[m] = base[(128 * m + t) * 2 + c];
    w1[1] = twl(t); w2[1] = twl(16 * b);
    const f2 tw = twl(2 * p);
    const int T = tid, Tm = (256 - T) & 255;
    const int colA = p, colB = p ? 512 - p : 256;
    u32 wa[8], wb[8];
    if (p) {
#pragma unroll
        for (int j = 0; j < 8; ++j) { wa[j] = Wt[colA * S_ + T + 256 * j]; wb[j] = Wt[colB * S_ + Tm + 256 * j]; }
    }
    twchain<16>(w1); twchain<16>(w2);
    dft16<-1>(v);
    f2* C = sm + c * CS;
    C[t] = v[0];
#pragma unroll
    for (int k = 1; k < 16; ++k) C[k * 136 + t] = cmul(v[k], w1[k]);
    __syncthreads();
#pragma unroll
    for (int a = 0; a < 16; ++a) v[a] = C[k1 * 136 + 8 * a + b];
    dft16<-1>(v);
    __syncthreads();
    C[b * 258 + k1] = v[0];
#pragma unroll
    for (int k = 1; k < 16; ++k) C[b * 258 + k1 + 16 * k] = cmul(v[k], w2[k]);
    __syncthreads();
    f2* A = v; f2* B = v + 8;
#pragma unroll
    for (int j = 0; j < 8; ++j) { A[j] = sm[j * 258 + T]; B[j] = sm[CS + j * 258 + Tm]; }
    dft8<-1>(A); dft8<-1>(B);
    if (p) {
        const f2 twi = f2{tw.y, -tw.x};
        if (T == 0) {
            f2 t0 = B[0]; u32 w0 = wb[0];
#pragma unroll
            for (int j = 0; j < 7; ++j) { B[j] = B[j + 1]; wb[j] = wb[j + 1]; }
            B[7] = t0; wb[7] = w0;
        }
#pragma unroll
        for (int j = 0; j < 8; ++j) {
            const f2 za = A[j], zb = B[7 - j];
            const f2 E = add_cj(za, zb), Dd = sub_cj(za, zb);
            const f2 wO = cmul(Dd, twi);
            const f2 Xa = E + wO, Xbc = E - wO;
            const f2 Ya = cmul(Xa, unpk(wa[j])), Ybc = cmulc(Xbc, unpk(wb[7 - j]));
            const f2 E2 = Ya + Ybc, O2 = cmulc(Ya - Ybc, tw);
            A[j] = add_ib(E2, O2);
            B[7 - j] = cj_sub_ib(E2, O2);
        }
        if (T == 0) {
            f2 t7 = B[7];
#pragma unroll
            for (int j = 7; j > 0; --j) B[j] = B[j - 1];
            B[0] = t7;
        }
    } else {
        __syncthreads();
#pragma unroll
        for (int j = 0; j < 8; ++j) { sm[T + 256 * j] = A[j]; sm[CS + Tm + 256 * j] = B[j]; }
        __syncthreads();
#pragma unroll
        for (int j = 0; j < 8; ++j) {
            {
                const int k = T + 256 * j, km = (2048 - k) & 2047;
                const f2 za = sm[k], zb = sm[km];
                const f2 E = add_cj(za, zb), Dd = sub_cj(za, zb);
                const f2 X0 = sub_ib(E, Dd), X5 = add_ib(E, Dd);
                const f2 y0 = cmul(X0, unpk(Wt[k])), y0m = cmulc(X0, unpk(Wt[km]));
                const f2 y5 = cmul(X5, unpk(Wt[512 * S_ + k])), y5m = cmulc(X5, unpk(Wt[512 * S_ + km]));
                const f2 s0 = (y0 + y0m) * 0.5f, s5 = (y5 + y5m) * 0.5f;
                A[j] = add_ib(s0 + s5, s0 - s5);
            }
            {
                const int k = Tm + 256 * j, km = (2048 - k) & 2047;
                const f2 za = sm[CS + k], zb = sm[CS + km];
                const f2 E = add_cj(za, zb), Dd = sub_cj(za, zb);
                const f2 Xa = E - Dd, Xbc = E + Dd;
                const f2 Ya = cmul(Xa, unpk(Wt[256 * S_ + k])), Ybc = cmulc(Xbc, unpk(Wt[256 * S_ + km]));
                const f2 E2 = Ya + Ybc, O2 = cmulc(Ya - Ybc, f2{0.f, -1.f});
                B[j] = add_ib(E2, O2);
            }
        }
    }
    dft8<1>(A); dft8<1>(B);
    __syncthreads();
#pragma unroll
    for (int j = 0; j < 8; ++j) { sm[j * 258 + T] = A[j]; sm[CS + j * 258 + Tm] = B[j]; }
    __syncthreads();
    v[0] = C[b * 258 + k1];
#pragma unroll
    for (int k = 1; k < 16; ++k) v[k] = cmulc(C[b * 258 + k1 + 16 * k], w2[k]);
    dft16<1>(v);
    __syncthreads();
#pragma unroll
    for (int a = 0; a < 16; ++a) C[k1 * 136 + 8 * a + b] = v[a];
    __syncthreads();
    v[0] = C[t];
#pragma unroll
    for (int k = 1; k < 16; ++k) v[k] = cmulc(C[k * 136 + t], w1[k]);
    dft16<1>(v);
#pragma unroll
    for (int m = 0; m < 16; ++m) base[(128 * m + t) * 2 + c] = v[m];
}

__global__ __launch_bounds__(512) void k3_ifft_ln(const f2* __restrict__ P, const u16* __restrict__ x, u16* __restrict__ h, u16* __restrict__ ln2) {
    __shared__ __attribute__((aligned(16))) f2 sm[8 * RS];
    const int tid = threadIdx.x, lane = tid & 63, wv = tid >> 6;
    const int s0 = blockIdx.x * 8, s = s0 + wv;
    const u32* xr = (const u32*)x + (size_t)s * 512;
    f32x4 q[4];
#pragma unroll
    for (int i = 0; i < 4; ++i) {
        const int it = tid + 512 * i, r = it & 7, p = it >> 3;
        q[i] = *(const f32x4*)(P + ((size_t)p * S_ + s0 + r) * 2);
    }
    u32 xraw[8];
#pragma unroll
    for (int m = 0; m < 8; ++m) xraw[m] = xr[lane + 64 * m];
    f2 v[8], w1[8], w2[8];
    w1[1] = twl(4 * lane); w2[1] = twl(32 * (lane & 7));
    twchain<8>(w1); twchain<8>(w2);
#pragma unroll
    for (int i = 0; i < 4; ++i) {
        const int it = tid + 512 * i, r = it & 7, p = it >> 3;
        const int ca = p, cb = p ? 512 - p : 256;
        sm[r * RS + ca] = f2{q[i][0], q[i][1]};
        sm[r * RS + cb] = f2{q[i][2], q[i][3]};
    }
    __syncthreads();
    f2* R = sm + wv * RS;
#pragma unroll
    for (int k = 0; k < 8; ++k) v[k] = R[lane + 64 * k];
    dft8<1>(v);
    __builtin_amdgcn_wave_barrier();
    {
        const int k1 = lane & 7, k2 = lane >> 3;
#pragma unroll
        for (int b = 0; b < 8; ++b) R[k1 * 72 + 9 * b + k2] = v[b];
    }
    __builtin_amdgcn_wave_barrier();
    {
        const int k1 = lane >> 3, b = lane & 7;
        v[0] = R[k1 * 72 + b * 9];
#pragma unroll
        for (int k = 1; k < 8; ++k) v[k] = cmulc(R[k1 * 72 + b * 9 + k], w2[k]);
        dft8<1>(v);
        __builtin_amdgcn_wave_barrier();
#pragma unroll
        for (int a = 0; a < 8; ++a) R[k1 * 72 + 8 * a + b] = v[a];
    }
    __builtin_amdgcn_wave_barrier();
    v[0] = R[lane];
#pragma unroll
    for (int k = 1; k < 8; ++k) v[k] = cmulc(R[k * 72 + lane], w1[k]);
    dft8<1>(v);
    u32* hr = (u32*)h + (size_t)s * 512;
    u32* lr = (u32*)ln2 + (size_t)s * 512;
    const float NRM = 1.0f / 4194304.0f;
#pragma unroll
    for (int m = 0; m < 8; ++m) {
        const f2 y = v[m] * NRM;
        const f2 hs = unpk(xraw[m]) + unpk(pack2(y.x, y.y));
        const u32 hb = pack2(hs.x, hs.y);
        hr[lane + 64 * m] = hb;
        v[m] = unpk(hb);
    }
    ln_row(v);
#pragma unroll
    for (int m = 0; m < 8; ++m) lr[lane + 64 * m] = pack2(v[m].x, v[m].y);
}

__device__ __forceinline__ void glds16(const void* g, LAS void* l) {
    unsigned sv; const unsigned lb = (unsigned)__builtin_amdgcn_readfirstlane((unsigned)(size_t)l);
    asm volatile("s_mov_b32 %0, m0\n\ts_mov_b32 m0, %2\n\ts_nop 0\n\tglobal_load_lds_dwordx4 %1, off\n\ts_mov_b32 m0, %0" : "=&s"(sv) : "v"(g), "s"(lb) : "memory");
}
typedef short v4i16_t __attribute__((ext_vector_type(4)));
__device__ __forceinline__ s16x4 trread(LAS char* p) { return __builtin_bit_cast(s16x4, __builtin_amdgcn_ds_read_tr16_b64_v4i16((LAS v4i16_t*)p)); }
__device__ __forceinline__ bf16x8 cat8(s16x4 a, s16x4 b) { return bf16x8{a[0], a[1], a[2], a[3], b[0], b[1], b[2], b[3]}; }
#define WAITV0() asm volatile("s_waitcnt vmcnt(0)" ::: "memory")

__device__ __forceinline__ float gelu_tanh(float x) {
    const float k0 = 0.7978845608028654f, k1 = 0.044715f;
    const float u = k0 * (x + k1 * x * x * x);
    const float e = __expf(-2.0f * u);
    return x / (1.0f + e);
}

__device__ __forceinline__ f2 geglu2(f2 g, f2 u) {
    const float c0 = -2.0f * 0.7978845608028654f * 1.4426950408889634f, c1 = c0 * 0.044715f;
    const f2 z = g * g;
    const f2 t = g * (z * c1 + c0);
    f2 e; e.x = __builtin_amdgcn_exp2f(t.x); e.y = __builtin_amdgcn_exp2f(t.y);
    const f2 d = e + 1.0f;
    f2 r; r.x = __builtin_amdgcn_rcpf(d.x); r.y = __builtin_amdgcn_rcpf(d.y);
    return (g * r) * u;
}
__device__ __forceinline__ f32x4 geglu4(f32x4 g, f32x4 u) {
    const f2 a = geglu2(f2{g[0], g[1]}, f2{u[0], u[1]}), b = geglu2(f2{g[2], g[3]}, f2{u[2], u[3]});
    return f32x4{a.x, a.y, b.x, b.y};
}

#define BAR() do { asm volatile("s_waitcnt lgkmcnt(0)" ::: "memory"); __builtin_amdgcn_s_barrier(); __builtin_amdgcn_sched_barrier(0); } while (0)
#define WAITV(n) asm volatile("s_waitcnt vmcnt(" #n ")" ::: "memory")
constexpr int G1_AB = 32768, G1_BB = 28672, G1_BOFF = 2 * G1_AB, NT1 = D_ / 64;
__global__ __launch_bounds__(512, 2) void k4_gemm1(const u16* __restrict__ A, const u16* __restrict__ Wg, const u16* __restrict__ Wu, u16* __restrict__ act) {
    __shared__ __attribute__((aligned(16))) char smem[2 * G1_AB + 3 * G1_BB];
    LAS char* lds = (LAS char*)smem;
    const int tid = threadIdx.x, lane = tid & 63, wv = __builtin_amdgcn_readfirstlane(tid >> 6);
    const int bid = blockIdx.x, xcd = bid & 7, loc = bid >> 3;
    const int mt = (xcd & 1) * 4 + (loc & 3), nt = (xcd >> 1) * 8 + (loc >> 2);
    const int row0 = mt * 256, col0 = nt * 112;
    const int grp = wv >> 2, w4 = wv & 3;
    const char* src[8];
    size_t kadv;
    if (grp == 0) {
#pragma unroll
        for (int j = 0; j < 8; ++j) {
            const int pc = w4 + 4 * j, r = pc * 8 + (lane >> 3), cp = lane & 7, cc = cp ^ (r & 7);
            src[j] = (const char*)(A + (size_t)(row0 + r) * D_ + cc * 8);
        }
        kadv = 128;
    } else {
#pragma unroll
        for (int j = 0; j < 8; ++j) {
            int pc = w4 + 4 * j; if (pc >= 28) pc = 0;
            const int ch = pc * 64 + lane, blk = ch >> 3, wi = ch & 7, q = wi >> 1, hf = wi & 1;
            const int kb = blk / 14, pos = blk % 14, nb = pos ^ ((kb >> 1) & 1), k = 4 * kb + q;
            src[j] = nb < 7 ? (const char*)(Wg + (size_t)k * FF + col0 + nb * 16 + hf * 8)
                            : (const char*)(Wu + (size_t)k * FF + col0 + (nb - 7) * 16 + (hf ^ 1) * 8);
        }
        kadv = (size_t)64 * FF * 2;
    }
    auto stageA = [&](int slot) {
        LAS char* b = lds + slot * G1_AB + w4 * 1024;
#pragma unroll
        for (int j = 0; j < 8; ++j) { glds16(src[j], b + j * 4096); src[j] += kadv; }
    };
    auto stageB = [&](int slot) {
        LAS char* b = lds + G1_BOFF + slot * G1_BB + w4 * 1024;
#pragma unroll
        for (int j = 0; j < 7; ++j) { glds16(src[j], b + j * 4096); src[j] += kadv; }
    };
    const int fr = lane & 15, fq = lane >> 4;
    const int aoff0 = (w4 * 64 + fr) * 128 + ((fq ^ (fr & 7)) << 4);
    const int aoff1 = (w4 * 64 + fr) * 128 + (((4 + fq) ^ (fr & 7)) << 4);
    int bo[7];
#pragma unroll
    for (int n = 0; n < 7; ++n) {
        const int j = grp * 7 + n, isup = (fr >> 1) & 1, blk = isup * 7 + (j >> 1);
        bo[n] = G1_BOFF + (2 * fq * 14 + (blk ^ (fq & 1))) * 128 + (fr >> 2) * 32 + (((j & 1) ^ isup) << 4) + (fr & 1) * 8;
    }
    f32x4 acc[4][7];
#pragma unroll
    for (int m = 0; m < 4; ++m)
#pragma unroll
        for (int n = 0; n < 7; ++n) acc[m][n] = f32x4{0.f, 0.f, 0.f, 0.f};
    bf16x8 af[2][4], bfr_[2][7];
    auto ldfr = [&](int aslot, int bslot) {
        LAS char* b = lds + bslot * G1_BB;
        LAS char* a = lds + aslot * G1_AB;
#pragma unroll
        for (int ks = 0; ks < 2; ++ks) {
#pragma unroll
            for (int n = 0; n < 7; ++n) {
                LAS char* pb = b + bo[n] + ks * (8 * 14 * 128);
                bfr_[ks][n] = cat8(trread(pb), trread(pb + 14 * 128));
            }
#pragma unroll
            for (int m = 0; m < 4; ++m) af[ks][m] = *(LAS bf16x8*)(a + (ks ? aoff1 : aoff0) + m * 2048);
        }
    };
    auto mma = [&]() {
        __builtin_amdgcn_s_setprio(1);
#pragma unroll
        for (int ks = 0; ks < 2; ++ks)
#pragma unroll
            for (int n = 0; n < 7; ++n)
#pragma unroll
                for (int m = 0; m < 4; ++m) acc[m][n] = __builtin_amdgcn_mfma_f32_16x16x32_bf16(bfr_[ks][n], af[ks][m], acc[m][n], 0, 0, 0);
        __builtin_amdgcn_s_setprio(0);
    };
    if (grp == 0) { stageA(0); WAITV(0); }
    else { stageB(0); stageB(1); WAITV(7); }
    BAR();
    if (grp == 0) {
        int bs = 0;
        for (int t = 0; t < NT1; ++t) {
            if (t + 1 < NT1) stageA((t + 1) & 1);
            ldfr(t & 1, bs);
            BAR();
            mma();
            WAITV(0);
            BAR();
            bs = bs == 2 ? 0 : bs + 1;
        }
    } else {
        BAR();
        int bs = 0;
        for (int t = 0; t < NT1; ++t) {
            const int b2 = bs == 0 ? 2 : bs - 1;
            if (t + 2 < NT1) stageB(b2);
            ldfr(t & 1, bs);
            if (t + 2 < NT1) { WAITV(7); } else { WAITV(0); }
            BAR();
            mma();
            if (t + 1 < NT1) BAR();
            bs = bs == 2 ? 0 : bs + 1;
        }
    }
    {
        char* ob = (char*)act + ((size_t)((row0 >> 4) + w4 * 4) * (FF / 8) + (col0 >> 3) + grp * 7) * 256 + fr * 16;
        auto grp4 = [&](f32x4& t0, f32x4& t1, f32x4& t2, f32x4& t3, size_t o0, size_t o1, size_t o2, size_t o3) {
            float gp[4], up[4], gq[4], uq[4];
#pragma unroll
            for (int r = 0; r < 4; ++r) {
                auto x = __builtin_amdgcn_permlane32_swap(__float_as_uint(t0[r]), __float_as_uint(t2[r]), false, false);
                gp[r] = __uint_as_float(x[0]); up[r] = __uint_as_float(x[1]);
                auto y = __builtin_amdgcn_permlane32_swap(__float_as_uint(t1[r]), __float_as_uint(t3[r]), false, false);
                gq[r] = __uint_as_float(y[0]); uq[r] = __uint_as_float(y[1]);
            }
            const f2 pa = geglu2(f2{gp[0], gp[1]}, f2{up[0], up[1]}), pb = geglu2(f2{gp[2], gp[3]}, f2{up[2], up[3]});
            const f2 qa = geglu2(f2{gq[0], gq[1]}, f2{uq[0], uq[1]}), qb = geglu2(f2{gq[2], gq[3]}, f2{uq[2], uq[3]});
            const u32 p0 = pack2(pa.x, pa.y), p1 = pack2(pb.x, pb.y), q0 = pack2(qa.x, qa.y), q1 = pack2(qb.x, qb.y);
            auto s0 = __builtin_amdgcn_permlane16_swap(p0, q0, false, false);
            auto s1 = __builtin_amdgcn_permlane16_swap(p1, q1, false, false);
            const size_t o = fq == 0 ? o0 : fq == 1 ? o1 : fq == 2 ? o2 : o3;
            *(u32x4*)(ob + o) = u32x4{s0[0], s1[0], s0[1], s1[1]};
        };
        constexpr size_t MS = (size_t)(FF / 8) * 256;
#pragma unroll
        for (int m = 0; m < 4; ++m)
            grp4(acc[m][0], acc[m][1], acc[m][2], acc[m][3], m * MS, m * MS + 256, m * MS + 512, m * MS + 768);
#pragma unroll
        for (int n = 4; n < 7; ++n)
            grp4(acc[0][n], acc[1][n], acc[2][n], acc[3][n], n * 256, MS + n * 256, 2 * MS + n * 256, 3 * MS + n * 256);
    }
}

constexpr int G2_U = 32768, NU2 = FF / 2 / 64;
__global__ __launch_bounds__(512, 2) void k5_gemm2(const u16* __restrict__ A, const u16* __restrict__ Wd, const u16* __restrict__ h,
                                                   u16* __restrict__ out, f32x4* __restrict__ part, u32* __restrict__ flags) {
    __shared__ __attribute__((aligned(16))) char smem[4 * G2_U];
    LAS char* lds = (LAS char*)smem;
    const int tid = threadIdx.x, lane = tid & 63, wv = __builtin_amdgcn_readfirstlane(tid >> 6);
    const int bid = blockIdx.x, xcd = bid & 7, loc = bid >> 3;
    const int mt = (xcd >> 1) * 4 + (loc & 3), nt = (xcd & 1) * 4 + ((loc >> 2) & 3), ksp = loc >> 4;
    const int row0 = mt * 128, col0 = nt * 128, kbase = ksp * (FF / 2);
    const int grp = wv >> 2, w4 = wv & 3;
    const char* src[8];
#pragma unroll
    for (int j = 0; j < 4; ++j) {
        const int pc = w4 + 4 * j, c = pc >> 1, r = (pc & 1) * 64 + lane;
        src[j] = (const char*)A + ((size_t)((row0 + r) >> 4) * (FF / 8) + (kbase >> 3) + c) * 256 + (r & 15) * 16;
    }
#pragma unroll
    for (int j = 0; j < 4; ++j) {
        const int pc = w4 + 4 * j, ch = pc * 64 + lane, blk = ch >> 3, wi = ch & 7, q = wi >> 1, hf = wi & 1;
        const int kb = blk >> 3, pos = blk & 7, nb = pos ^ ((kb >> 1) & 1), k = 4 * kb + q;
        src[4 + j] = (const char*)(Wd + (size_t)(kbase + k) * D_ + col0 + nb * 16 + hf * 8);
    }
    auto stage = [&](int u) {
        LAS char* b = lds + (u & 3) * G2_U + w4 * 1024;
        const size_t ao = (size_t)u * 8 * 256, bo = (size_t)u * 64 * D_ * 2;
#pragma unroll
        for (int j = 0; j < 4; ++j) glds16(src[j] + ao, b + j * 4096);
#pragma unroll
        for (int j = 0; j < 4; ++j) glds16(src[4 + j] + bo, b + 16384 + j * 4096);
    };
    const int fr = lane & 15, fq = lane >> 4, wr = w4 >> 1, wc = w4 & 1;
    const int aoff = fq * 2048 + (wr * 64 + fr) * 16;
    const int bl = 16384 + (2 * fq * 8) * 128 + (fr >> 2) * 32 + (fr & 3) * 8 + wc * 4 * 128;
    const int sgn = (fq & 1) * 128;
    const int bo0 = bl + sgn, bo1 = bl - sgn;
    f32x4 acc[4][4];
#pragma unroll
    for (int m = 0; m < 4; ++m)
#pragma unroll
        for (int n = 0; n < 4; ++n) acc[m][n] = f32x4{0.f, 0.f, 0.f, 0.f};
    bf16x8 af[2][4], bfr_[2][4];
    auto ldfr = [&](int u) {
        LAS char* b = lds + (u & 3) * G2_U;
#pragma unroll
        for (int ks = 0; ks < 2; ++ks) {
#pragma unroll
            for (int n = 0; n < 4; ++n) {
                LAS char* pb = b + ((n & 1) ? bo1 : bo0) + n * 128 + ks * (8 * 8 * 128);
                bfr_[ks][n] = cat8(trread(pb), trread(pb + 8 * 128));
            }
#pragma unroll
            for (int m = 0; m < 4; ++m) af[ks][m] = *(LAS bf16x8*)(b + aoff + ks * 8192 + m * 256);
        }
    };
    auto mma = [&]() {
        __builtin_amdgcn_s_setprio(1);
#pragma unroll
        for (int ks = 0; ks < 2; ++ks)
#pragma unroll
            for (int n = 0; n < 4; ++n)
#pragma unroll
                for (int m = 0; m < 4; ++m) acc[m][n] = __builtin_amdgcn_mfma_f32_16x16x32_bf16(bfr_[ks][n], af[ks][m], acc[m][n], 0, 0, 0);
        __builtin_amdgcn_s_setprio(0);
    };
    if (grp == 0) { stage(0); stage(2); WAITV(8); }
    else { stage(1); }
    BAR();
    if (grp == 0) {
        for (int u = 0; u < NU2; u += 2) {
            if (u + 3 < NU2) stage(u + 3);
            ldfr(u);
            if (u + 3 < NU2) { WAITV(8); } else { WAITV(0); }
            BAR();
            mma();
            BAR();
        }
    } else {
        WAITV(0);
        BAR();
        for (int u = 1; u < NU2; u += 2) {
            if (u + 3 < NU2) stage(u + 3);
            ldfr(u);
            if (u + 3 < NU2) { WAITV(8); } else { WAITV(0); }
            BAR();
            mma();
            if (u + 1 < NU2) BAR();
        }
    }
    __syncthreads();
    LAS f32x4* ex = (LAS f32x4*)lds + w4 * 16 * 64 + lane;
    if (grp == 1) {
#pragma unroll
        for (int m = 0; m < 4; ++m)
#pragma unroll
            for (int n = 0; n < 4; ++n) ex[(m * 4 + n) * 64] = acc[m][n];
    }
    __syncthreads();
    const int tile = mt * 8 + nt;
    if (grp == 0) {
#pragma unroll
        for (int m = 0; m < 4; ++m)
#pragma unroll
            for (int n = 0; n < 4; ++n) acc[m][n] += ex[(m * 4 + n) * 64];
        if (wr != ksp) {
            __amdgpu_buffer_rsrc_t rs = __builtin_amdgcn_make_buffer_rsrc((void*)part, 0, 0x7fffffff, 0x00020000);
            const int off = (((tile * 2 + ksp) * 2 + wc) * 16 * 64 + lane) * 16;
#pragma unroll
            for (int m = 0; m < 4; ++m)
#pragma unroll
                for (int n = 0; n < 4; ++n)
                    __builtin_amdgcn_raw_buffer_store_b128(__builtin_bit_cast(u32x4, acc[m][n]), rs, off + (m * 4 + n) * 1024, 0, 16);
            WAITV(0);
        }
    }
    __syncthreads();
    GAS u32* fl = (GAS u32*)flags;
    if (tid == 0) __hip_atomic_store(fl + (tile * 2 + ksp) * 16, 1u, __ATOMIC_RELAXED, __HIP_MEMORY_SCOPE_AGENT);
    if (grp == 0 && wr == ksp) {
        u32x2 hv[4][4];
        const size_t ro = (size_t)(row0 + wr * 64 + fr) * D_ + col0 + wc * 64 + fq * 4;
#pragma unroll
        for (int m = 0; m < 4; ++m)
#pragma unroll
            for (int n = 0; n < 4; ++n) hv[m][n] = *(const u32x2*)(h + ro + (size_t)m * 16 * D_ + n * 16);
        unsigned spins = 0;
        while (__builtin_amdgcn_readfirstlane(__hip_atomic_load(fl + (tile * 2 + (ksp ^ 1)) * 16, __ATOMIC_RELAXED, __HIP_MEMORY_SCOPE_AGENT)) == 0u) {
            __builtin_amdgcn_s_sleep(1);
            if (++spins > (1u << 22)) break;
        }
        __amdgpu_buffer_rsrc_t rs = __builtin_amdgcn_make_buffer_rsrc((void*)part, 0, 0x7fffffff, 0x00020000);
        const int toff = (((tile * 2 + (ksp ^ 1)) * 2 + wc) * 16 * 64 + lane) * 16;
        f32x4 o[4][4];
#pragma unroll
        for (int m = 0; m < 4; ++m)
#pragma unroll
            for (int n = 0; n < 4; ++n) o[m][n] = __builtin_bit_cast(f32x4, __builtin_amdgcn_raw_buffer_load_b128(rs, toff + (m * 4 + n) * 1024, 0, 16));
#pragma unroll
        for (int m = 0; m < 4; ++m)
#pragma unroll
            for (int n = 0; n < 4; ++n) {
                const f32x4 sum = acc[m][n] + o[m][n];
                float r[4];
#pragma unroll
                for (int j = 0; j < 4; ++j) {
                    const u32 hw = hv[m][n][j >> 1];
                    r[j] = bf2f((j & 1) ? (hw >> 16) : (hw & 0xffffu)) + bfr(sum[j]);
                }
                *(u32x2*)(out + ro + (size_t)m * 16 * D_ + n * 16) = u32x2{pack2(r[0], r[1]), pack2(r[2], r[3])};
            }
    }
}

extern "C" void kernel_launch(void* const* d_in, const int* in_sizes, int n_in, void* d_out, int out_size, void* d_ws, size_t ws_size,
                              hipStream_t stream) {
    const u16* x = (const u16*)d_in[0];
    const u16* wr = (const u16*)d_in[1];
    const u16* wi = (const u16*)d_in[2];
    const u16* wg = (const u16*)d_in[4];
    const u16* wu = (const u16*)d_in[5];
    const u16* wd = (const u16*)d_in[6];
    u16* out = (u16*)d_out;
    char* ws = (char*)d_ws;
    f2* P = (f2*)(ws);
    u32* Wt = (u32*)(ws + (8u << 20));
    u16* h = (u16*)(ws + (13u << 20));
    u16* ln2 = (u16*)(ws + (17u << 20));
    u16* act = (u16*)(ws + (21u << 20));
    f32x4* part = (f32x4*)(ws + (36u << 20));
    u32* flags = (u32*)(ws + (70u << 20));
    k1_ln_fft<<<256 + 288, 512, 0, stream>>>(x, P, wr, wi, Wt, flags);
    k2_col<<<256, 256, 0, stream>>>(P, Wt);
    k3_ifft_ln<<<256, 512, 0, stream>>>(P, x, h, ln2);
    k4_gemm1<<<256, 512, 0, stream>>>(ln2, wg, wu, act);
    k5_gemm2<<<256, 512, 0, stream>>>(act, wd, h, out, part, flags);
}
```

```cpp
#include <hip/hip_runtime.h>
#include <hip/hip_bf16.h>

typedef unsigned short u16;
typedef unsigned int u32;
typedef float f2 __attribute__((ext_vector_type(2)));
typedef float f32x4 __attribute__((ext_vector_type(4)));
typedef short bf16x8 __attribute__((ext_vector_type(8)));
typedef short s16x4 __attribute__((ext_vector_type(4)));
typedef unsigned u32x4 __attribute__((ext_vector_type(4)));
typedef unsigned u32x2 __attribute__((ext_vector_type(2)));
#define LAS __attribute__((address_space(3)))
#define GAS __attribute__((address_space(1)))

constexpr int S_ = 2048, D_ = 1024, DF = 513, FF = 3584;

__device__ __forceinline__ float bf2f(u32 h) { return __uint_as_float(h << 16); }
__device__ __forceinline__ u32 f2bf(float f) { __bf16 b = (__bf16)f; return (u32)__builtin_bit_cast(u16, b); }
__device__ __forceinline__ float bfr(float f) { return bf2f(f2bf(f)); }
typedef __bf16 bf16x2_t __attribute__((ext_vector_type(2)));
__device__ __forceinline__ u32 pack2(float lo, float hi) { return __builtin_bit_cast(u32, __builtin_convertvector(f2{lo, hi}, bf16x2_t)); }

__device__ __forceinline__ f2 add_ib(f2 a, f2 b) {
    f2 r; asm("v_pk_add_f32 %0, %1, %2 op_sel:[0,1] op_sel_hi:[1,0] neg_lo:[0,1]" : "=v"(r) : "v"(a), "v"(b)); return r; }
__device__ __forceinline__ f2 sub_ib(f2 a, f2 b) {
    f2 r; asm("v_pk_add_f32 %0, %1, %2 op_sel:[0,1] op_sel_hi:[1,0] neg_hi:[0,1]" : "=v"(r) : "v"(a), "v"(b)); return r; }
__device__ __forceinline__ f2 add_cj(f2 a, f2 b) {
    f2 r; asm("v_pk_add_f32 %0, %1, %2 neg_hi:[0,1]" : "=v"(r) : "v"(a), "v"(b)); return r; }
__device__ __forceinline__ f2 sub_cj(f2 a, f2 b) {
    f2 r; asm("v_pk_add_f32 %0, %1, %2 neg_lo:[0,1]" : "=v"(r) : "v"(a), "v"(b)); return r; }
__device__ __forceinline__ f2 cj_sub_ib(f2 a, f2 b) {
    f2 r; asm("v_pk_add_f32 %0, %1, %2 op_sel:[0,1] op_sel_hi:[1,0] neg_hi:[1,0]" : "=v"(r) : "v"(a), "v"(b)); return r; }
__device__ __forceinline__ f2 cmul(f2 a, f2 w) {
    f2 t, r;
    asm("v_pk_mul_f32 %0, %1, %2 op_sel:[0,0] op_sel_hi:[0,1]" : "=v"(t) : "v"(a), "v"(w));
    asm("v_pk_fma_f32 %0, %1, %2, %3 op_sel:[1,1,0] op_sel_hi:[1,0,1] neg_lo:[0,1,0]" : "=v"(r) : "v"(a), "v"(w), "v"(t));
    return r; }
__device__ __forceinline__ f2 cmulc(f2 a, f2 w) {
    f2 t, r;
    asm("v_pk_mul_f32 %0, %1, %2 op_sel:[0,0] op_sel_hi:[0,1] neg_hi:[0,1]" : "=v"(t) : "v"(a), "v"(w));
    asm("v_pk_fma_f32 %0, %1, %2, %3 op_sel:[1,1,0] op_sel_hi:[1,0,1]" : "=v"(r) : "v"(a), "v"(w), "v"(t));
    return r; }
template <int D> __device__ __forceinline__ f2 twm(f2 a, f2 w) { return D > 0 ? cmulc(a, w) : cmul(a, w); }
template <int D> __device__ __forceinline__ f2 add_di(f2 a, f2 b) { return D > 0 ? add_ib(a, b) : sub_ib(a, b); }
template <int D> __device__ __forceinline__ f2 sub_di(f2 a, f2 b) { return D > 0 ? sub_ib(a, b) : add_ib(a, b); }

template <int D> __device__ __forceinline__ void dft4(f2& x0, f2& x1, f2& x2, f2& x3) {
    const f2 t0 = x0 + x2, t1 = x0 - x2, t2 = x1 + x3, d = x1 - x3;
    x0 = t0 + t2; x2 = t0 - t2; x1 = add_di<D>(t1, d); x3 = sub_di<D>(t1, d);
}
template <int D> __device__ __forceinline__ void dft8(f2* x) {
    dft4<D>(x[0], x[2], x[4], x[6]);
    dft4<D>(x[1], x[3], x[5], x[7]);
    const float c = 0.70710678118654752440f;
    const f2 o1 = add_di<D>(x[3], x[3]) * c;
    const f2 o3 = sub_di<D>(x[7], x[7]) * c;
    const f2 e0 = x[0], e1 = x[2], e2 = x[4], e3 = x[6], o0 = x[1], o2 = x[5];
    x[0] = e0 + o0; x[4] = e0 - o0;
    x[1] = e1 + o1; x[5] = e1 - o1;
    x[2] = add_di<D>(e2, o2); x[6] = sub_di<D>(e2, o2);
    x[3] = e3 - o3; x[7] = e3 + o3;
}
template <int D> __device__ __forceinline__ void dft16(f2* x) {
    f2 e[8], o[8];
#pragma unroll
    for (int i = 0; i < 8; ++i) { e[i] = x[2 * i]; o[i] = x[2 * i + 1]; }
    dft8<D>(e); dft8<D>(o);
    const float c1 = 0.92387953251128675613f, s1 = 0.38268343236508977173f, c2 = 0.70710678118654752440f;
    o[1] = twm<D>(o[1], f2{c1, -s1});
    o[2] = add_di<D>(o[2], o[2]) * c2;
    o[3] = twm<D>(o[3], f2{s1, -c1});
    o[5] = twm<D>(o[5], f2{-s1, -c1});
    o[6] = sub_di<D>(o[6], o[6]) * c2;
    o[7] = twm<D>(o[7], f2{-c1, -s1});
#pragma unroll
    for (int i = 0; i < 8; ++i) {
        if (i == 4) { x[4] = add_di<D>(e[4], o[4]); x[12] = sub_di<D>(e[4], o[4]); }
        else if (i == 6) { x[6] = e[6] - o[6]; x[14] = e[6] + o[6]; }
        else { x[i] = e[i] + o[i]; x[i + 8] = e[i] - o[i]; }
    }
}
struct TwTab { float v[4096]; };
constexpr double c_sin(double x) { double t = x, r = x; for (int i = 1; i < 12; ++i) { t *= -x * x / ((2 * i) * (2 * i + 1)); r += t; } return r; }
constexpr double c_cos(double x) { double t = 1, r = 1; for (int i = 1; i < 12; ++i) { t *= -x * x / ((2 * i - 1) * (2 * i)); r += t; } return r; }
constexpr TwTab make_tw() {
    TwTab t{};
    const double step = 6.283185307179586476925286766559 / 2048.0;
    for (int k = 0; k < 2048; ++k) {
        const int q = k >> 9, r = k & 511;
        double c = 0, s = 0;
        if (r <= 256) { c = c_cos(r * step); s = c_sin(r * step); } else { c = c_sin((512 - r) * step); s = c_cos((512 - r) * step); }
        double cc = q == 0 ? c : q == 1 ? -s : q == 2 ? -c : s;
        double ss = q == 0 ? s : q == 1 ? c : q == 2 ? -s : -c;
        t.v[2 * k] = (float)cc; t.v[2 * k + 1] = (float)(-ss);
    }
    return t;
}
__device__ const TwTab TW = make_tw();
__device__ __forceinline__ f2 twl(int k) { return *(const f2*)(TW.v + 2 * k); }
template <int N> __device__ __forceinline__ void twchain(f2* w) {
#pragma unroll
    for (int k = 2; k < N; ++k) w[k] = (k & 1) ? cmul(w[k - 1], w[1]) : cmul(w[k / 2], w[k / 2]);
}
__device__ __forceinline__ float wave_sum(float v) {
#pragma unroll
    for (int o = 32; o >= 1; o >>= 1) v += __shfl_xor(v, o);
    return v;
}
__device__ __forceinline__ void ln_row(f2* x) {
    f2 s2 = (x[0] + x[1]) + (x[2] + x[3]) + ((x[4] + x[5]) + (x[6] + x[7]));
    const float mean = wave_sum(s2.x + s2.y) * (1.0f / 1024.0f);
    f2 q2 = f2{0.f, 0.f};
#pragma unroll
    for (int m = 0; m < 8; ++m) { const f2 d = x[m] - mean; q2 += d * d; }
    const float var = wave_sum(q2.x + q2.y) * (1.0f / 1024.0f);
    const float mb = bfr(mean);
    const float rden = 1.0f / bfr(sqrtf(bfr(bfr(var) + 1e-5f)));
#pragma unroll
    for (int m = 0; m < 8; ++m) {
        const f2 d = x[m] - mb;
        const u32 db = pack2(d.x, d.y);
        const f2 q = f2{bf2f(db & 0xffffu), __uint_as_float(db & 0xffff0000u)} * rden;
        const u32 qb = pack2(q.x, q.y);
        x[m] = f2{bf2f(qb & 0xffffu), __uint_as_float(qb & 0xffff0000u)};
    }
}

constexpr int RS = 580;
__global__ __launch_bounds__(512) void k1_ln_fft(const u16* __restrict__ x, f2* __restrict__ P, const u16* __restrict__ wr,
                                                 const u16* __restrict__ wi, u32* __restrict__ Wt, u32* __restrict__ flags) {
    __shared__ __attribute__((aligned(16))) f2 sm[8 * RS];
    const int tid = threadIdx.x, lane = tid & 63, wv = tid >> 6;
    if (blockIdx.x >= 256) {
        const int b = blockIdx.x - 256;
        if (b == 0 && tid < 256) flags[tid * 16] = 0u;
        u32* tile = (u32*)sm;
        const int k0 = (b & 31) * 64, c0 = (b >> 5) * 64;
#pragma unroll
        for (int i = 0; i < 8; ++i) {
            const int r = wv + 8 * i, c = c0 + lane;
            u32 v = 0;
            if (c < DF) v = (u32)wr[(k0 + r) * DF + c] | ((u32)wi[(k0 + r) * DF + c] << 16);
            tile[r * 65 + lane] = v;
        }
        __syncthreads();
#pragma unroll
        for (int i = 0; i < 8; ++i) {
            const int c = wv + 8 * i;
            if (c0 + c < DF) Wt[(c0 + c) * S_ + k0 + lane] = tile[lane * 65 + c];
        }
        return;
    }
    const int s = blockIdx.x * 8 + wv;
    const u32* xr = (const u32*)x + (size_t)s * 512;
    f2 v[8], w1[8], w2[8];
    u32 raw[8];
#pragma unroll
    for (int m = 0; m < 8; ++m) raw[m] = xr[lane + 64 * m];
    w1[1] = twl(4 * lane); w2[1] = twl(32 * (lane & 7));
#pragma unroll
    for (int m = 0; m < 8; ++m) v[m] = f2{bf2f(raw[m] & 0xffffu), __uint_as_float(raw[m] & 0xffff0000u)};
    ln_row(v);
    f2* R = sm + wv * RS;
    dft8<-1>(v);
    twchain<8>(w1);
    R[lane] = v[0];
#pragma unroll
    for (int k = 1; k < 8; ++k) R[k * 72 + lane] = cmul(v[k], w1[k]);
    __builtin_amdgcn_wave_barrier();
    {
        const int k1 = lane >> 3, b = lane & 7;
#pragma unroll
        for (int a = 0; a < 8; ++a) v[a] = R[k1 * 72 + 8 * a + b];
        dft8<-1>(v);
        twchain<8>(w2);
        __builtin_amdgcn_wave_barrier();
        R[k1 * 72 + b * 9] = v[0];
#pragma unroll
        for (int k = 1; k < 8; ++k) R[k1 * 72 + b * 9 + k] = cmul(v[k], w2[k]);
    }
    __builtin_amdgcn_wave_barrier();
    {
        const int k1 = lane & 7, k2 = lane >> 3;
#pragma unroll
        for (int b = 0; b < 8; ++b) v[b] = R[k1 * 72 + 9 * b + k2];
        dft8<-1>(v);
    }
    __builtin_amdgcn_wave_barrier();
#pragma unroll
    for (int k = 0; k < 8; ++k) R[lane + 64 * k] = v[k];
    __syncthreads();
    const int s0 = blockIdx.x * 8;
#pragma unroll
    for (int i = 0; i < 4; ++i) {
        const int it = tid + 512 * i, r = it & 7, p = it >> 3;
        const int ca = p, cb = p ? 512 - p : 256;
        f2 a = sm[r * RS + ca], b = sm[r * RS + cb];
        *(f32x4*)(P + ((size_t)p * S_ + s0 + r) * 2) = f32x4{a.x, a.y, b.x, b.y};
    }
}

__device__ __forceinline__ void glds16(const void* g, LAS void* l) {
    unsigned sv; const unsigned lb = (unsigned)__builtin_amdgcn_readfirstlane((unsigned)(size_t)l);
    asm volatile("s_mov_b32 %0, m0\n\ts_mov_b32 m0, %2\n\ts_nop 0\n\tglobal_load_lds_dwordx4 %1, off\n\ts_mov_b32 m0, %0" : "=&s"(sv) : "v"(g), "s"(lb) : "memory");
}
constexpr int CS = 2192;
__device__ __forceinline__ f2 unpk(u32 u) { return f2{bf2f(u & 0xffffu), __uint_as_float(u & 0xffff0000u)}; }
__device__ __forceinline__ void pair_gen(f2& za, f2& zb, u32 fa, u32 fb, f2 tw, f2 twi) {
    const f2 E = add_cj(za, zb), Dd = sub_cj(za, zb);
    const f2 wO = cmul(Dd, twi);
    const f2 Xa = E + wO, Xbc = E - wO;
    const f2 Ya = cmul(Xa, unpk(fa)), Ybc = cmulc(Xbc, unpk(fb));
    const f2 E2 = Ya + Ybc, O2 = cmulc(Ya - Ybc, tw);
    za = add_ib(E2, O2);
    zb = cj_sub_ib(E2, O2);
}
__device__ __forceinline__ void pair_zero(f2& za, f2& zb, u32 f0a, u32 f0b, u32 f5a, u32 f5b) {
    const f2 E = add_cj(za, zb), Dd = sub_cj(za, zb);
    const f2 X0 = sub_ib(E, Dd), X5 = add_ib(E, Dd);
    const f2 s0 = (cmul(X0, unpk(f0a)) + cmulc(X0, unpk(f0b))) * 0.5f;
    const f2 s5 = (cmul(X5, unpk(f5a)) + cmulc(X5, unpk(f5b))) * 0.5f;
    const f2 E2 = s0 + s5, O2 = s0 - s5;
    za = add_ib(E2, O2);
    zb = cj_sub_ib(E2, O2);
}
__global__ __launch_bounds__(256) void k2_col(f2* __restrict__ P, const u32* __restrict__ Wt, const u16* __restrict__ wg, const u16* __restrict__ wu,
                                              const u16* __restrict__ wd_) {
    __shared__ __attribute__((aligned(16))) f2 sm[2 * CS + 512];
    const int p = blockIdx.x, tid = threadIdx.x;
    const int c = tid & 1, t = tid >> 1, b = t & 7, k1 = t >> 3;
    f2* base = P + (size_t)p * 4096;
    f2 v[16], w1[16], w2[16];
#pragma unroll
    for (int m = 0; m < 16; ++m) v[m] = base[(128 * m + t) * 2 + c];
    w1[1] = twl(t); w2[1] = twl(16 * b);
    const int T = tid, tt = T & 127, cz = T >> 7;
    const int ka = p ? T : tt, kb = p ? ((256 - T) & 255) : (tt ? 256 - tt : 128);
    const int sa = p ? 0 : cz, sb = p ? 1 : cz;
    const int colA = p ? p : cz * 256, colB = p ? 512 - p : cz * 256;
    const f2 tw = twl(2 * colA);
    u32 wa[8], wb[8], wc[8], wd[8];
#pragma unroll
    for (int j = 0; j < 8; ++j) { wa[j] = Wt[colA * S_ + ka + 256 * j]; wb[j] = Wt[colB * S_ + kb + 256 * j]; }
    if (p == 0) {
#pragma unroll
        for (int j = 0; j < 8; ++j) { wc[j] = Wt[512 * S_ + ka + 256 * j]; wd[j] = Wt[512 * S_ + kb + 256 * j]; }
    }
    twchain<16>(w1); twchain<16>(w2);
    dft16<-1>(v);
    f2* C = sm + c * CS;
    C[t] = v[0];
#pragma unroll
    for (int k = 1; k < 16; ++k) C[k * 136 + t] = cmul(v[k], w1[k]);
    __syncthreads();
    {
        LAS char* scr = (LAS char*)(sm + 2 * CS) + (tid >> 6) * 1024;
        const size_t o = ((size_t)p * 256 + tid) * 16;
#pragma unroll
        for (int i = 0; i < 7; ++i) {
            glds16((const char*)wg + o + (size_t)i * (65536 * 16), scr);
            glds16((const char*)wu + o + (size_t)i * (65536 * 16), scr);
            glds16((const char*)wd_ + o + (size_t)i * (65536 * 16), scr);
        }
    }
#pragma unroll
    for (int a = 0; a < 16; ++a) v[a] = C[k1 * 136 + 8 * a + b];
    dft16<-1>(v);
    __syncthreads();
    C[b * 258 + k1] = v[0];
#pragma unroll
    for (int k = 1; k < 16; ++k) C[b * 258 + k1 + 16 * k] = cmul(v[k], w2[k]);
    __syncthreads();
    f2* A = v; f2* B = v + 8;
    f2* pa = sm + sa * CS + ka; f2* pb = sm + sb * CS + kb;
#pragma unroll
    for (int j = 0; j < 8; ++j) { A[j] = pa[j * 258]; B[j] = pb[j * 258]; }
    dft8<-1>(A); dft8<-1>(B);
    const f2 twi = f2{tw.y, -tw.x};
    if (p) {
        if (T == 0) {
            f2 t0 = B[0]; u32 w0 = wb[0];
#pragma unroll
            for (int j = 0; j < 7; ++j) { B[j] = B[j + 1]; wb[j] = wb[j + 1]; }
            B[7] = t0; wb[7] = w0;
        }
#pragma unroll
        for (int j = 0; j < 8; ++j) pair_gen(A[j], B[7 - j], wa[j], wb[7 - j], tw, twi);
        if (T == 0) {
            f2 t7 = B[7];
#pragma unroll
            for (int j = 7; j > 0; --j) B[j] = B[j - 1];
            B[0] = t7;
        }
    } else if (tt) {
        if (cz) {
#pragma unroll
            for (int j = 0; j < 8; ++j) pair_gen(A[j], B[7 - j], wa[j], wb[7 - j], tw, twi);
        } else {
#pragma unroll
            for (int j = 0; j < 8; ++j) pair_zero(A[j], B[7 - j], wa[j], wb[7 - j], wc[j], wd[7 - j]);
        }
    } else {
        f2 An[8];
#pragma unroll
        for (int j = 0; j < 8; ++j) {
            f2 za = A[j], zb = A[(8 - j) & 7];
            if (cz) pair_gen(za, zb, wa[j], wa[(8 - j) & 7], tw, twi);
            else pair_zero(za, zb, wa[j], wa[(8 - j) & 7], wc[j], wc[(8 - j) & 7]);
            An[j] = za;
        }
#pragma unroll
        for (int j = 0; j < 8; ++j) A[j] = An[j];
#pragma unroll
        for (int j = 0; j < 4; ++j) {
            if (cz) pair_gen(B[j], B[7 - j], wb[j], wb[7 - j], tw, twi);
            else pair_zero(B[j], B[7 - j], wb[j], wb[7 - j], wd[j], wd[7 - j]);
        }
    }
    dft8<1>(A); dft8<1>(B);
    __syncthreads();
#pragma unroll
    for (int j = 0; j < 8; ++j) { pa[j * 258] = A[j]; pb[j * 258] = B[j]; }
    __syncthreads();
    v[0] = C[b * 258 + k1];
#pragma unroll
    for (int k = 1; k < 16; ++k) v[k] = cmulc(C[b * 258 + k1 + 16 * k], w2[k]);
    dft16<1>(v);
    __syncthreads();
#pragma unroll
    for (int a = 0; a < 16; ++a) C[k1 * 136 + 8 * a + b] = v[a];
    __syncthreads();
    v[0] = C[t];
#pragma unroll
    for (int k = 1; k < 16; ++k) v[k] = cmulc(C[k * 136 + t], w1[k]);
    dft16<1>(v);
#pragma unroll
    for (int m = 0; m < 16; ++m) base[(128 * m + t) * 2 + c] = v[m];
    asm volatile("s_waitcnt vmcnt(0)" ::: "memory");
}

__global__ __launch_bounds__(512) void k3_ifft_ln(const f2* __restrict__ P, const u16* __restrict__ x, u16* __restrict__ h, u16* __restrict__ ln2) {
    __shared__ __attribute__((aligned(16))) f2 sm[8 * RS];
    const int tid = threadIdx.x, lane = tid & 63, wv = tid >> 6;
    const int s0 = blockIdx.x * 8, s = s0 + wv;
    const u32* xr = (const u32*)x + (size_t)s * 512;
    f32x4 q[4];
#pragma unroll
    for (int i = 0; i < 4; ++i) {
        const int it = tid + 512 * i, r = it & 7, p = it >> 3;
        q[i] = *(const f32x4*)(P + ((size_t)p * S_ + s0 + r) * 2);
    }
    u32 xraw[8];
#pragma unroll
    for (int m = 0; m < 8; ++m) xraw[m] = xr[lane + 64 * m];
    f2 v[8], w1[8], w2[8];
    w1[1] = twl(4 * lane); w2[1] = twl(32 * (lane & 7));
    twchain<8>(w1); twchain<8>(w2);
#pragma unroll
    for (int i = 0; i < 4; ++i) {
        const int it = tid + 512 * i, r = it & 7, p = it >> 3;
        const int ca = p, cb = p ? 512 - p : 256;
        sm[r * RS + ca] = f2{q[i][0], q[i][1]};
        sm[r * RS + cb] = f2{q[i][2], q[i][3]};
    }
    __syncthreads();
    f2* R = sm + wv * RS;
#pragma unroll
    for (int k = 0; k < 8; ++k) v[k] = R[lane + 64 * k];
    dft8<1>(v);
    __builtin_amdgcn_wave_barrier();
    {
        const int k1 = lane & 7, k2 = lane >> 3;
#pragma unroll
        for (int b = 0; b < 8; ++b) R[k1 * 72 + 9 * b + k2] = v[b];
    }
    __builtin_amdgcn_wave_barrier();
    {
        const int k1 = lane >> 3, b = lane & 7;
        v[0] = R[k1 * 72 + b * 9];
#pragma unroll
        for (int k = 1; k < 8; ++k) v[k] = cmulc(R[k1 * 72 + b * 9 + k], w2[k]);
        dft8<1>(v);
        __builtin_amdgcn_wave_barrier();
#pragma unroll
        for (int a = 0; a < 8; ++a) R[k1 * 72 + 8 * a + b] = v[a];
    }
    __builtin_amdgcn_wave_barrier();
    v[0] = R[lane];
#pragma unroll
    for (int k = 1; k < 8; ++k) v[k] = cmulc(R[k * 72 + lane], w1[k]);
    dft8<1>(v);
    u32* hr = (u32*)h + (size_t)s * 512;
    u32* lr = (u32*)ln2 + (size_t)s * 512;
    const float NRM = 1.0f / 4194304.0f;
#pragma unroll
    for (int m = 0; m < 8; ++m) {
        const f2 y = v[m] * NRM;
        const f2 hs = unpk(xraw[m]) + unpk(pack2(y.x, y.y));
        const u32 hb = pack2(hs.x, hs.y);
        hr[lane + 64 * m] = hb;
        v[m] = unpk(hb);
    }
    ln_row(v);
#pragma unroll
    for (int m = 0; m < 8; ++m) lr[lane + 64 * m] = pack2(v[m].x, v[m].y);
}

typedef short v4i16_t __attribute__((ext_vector_type(4)));
__device__ __forceinline__ s16x4 trread(LAS char* p) { return __builtin_bit_cast(s16x4, __builtin_amdgcn_ds_read_tr16_b64_v4i16((LAS v4i16_t*)p)); }
__device__ __forceinline__ bf16x8 cat8(s16x4 a, s16x4 b) { return bf16x8{a[0], a[1], a[2], a[3], b[0], b[1], b[2], b[3]}; }
#define WAITV0() asm volatile("s_waitcnt vmcnt(0)" ::: "memory")

__device__ __forceinline__ float gelu_tanh(float x) {
    const float k0 = 0.7978845608028654f, k1 = 0.044715f;
    const float u = k0 * (x + k1 * x * x * x);
    const float e = __expf(-2.0f * u);
    return x / (1.0f + e);
}

__device__ __forceinline__ f2 geglu2(f2 g, f2 u) {
    const float c0 = -2.0f * 0.7978845608028654f * 1.4426950408889634f, c1 = c0 * 0.044715f;
    const f2 z = g * g;
    const f2 t = g * (z * c1 + c0);
    f2 e; e.x = __builtin_amdgcn_exp2f(t.x); e.y = __builtin_amdgcn_exp2f(t.y);
    const f2 d = e + 1.0f;
    f2 r; r.x = __builtin_amdgcn_rcpf(d.x); r.y = __builtin_amdgcn_rcpf(d.y);
    return (g * r) * u;
}
__device__ __forceinline__ f32x4 geglu4(f32x4 g, f32x4 u) {
    const f2 a = geglu2(f2{g[0], g[1]}, f2{u[0], u[1]}), b = geglu2(f2{g[2], g[3]}, f2{u[2], u[3]});
    return f32x4{a.x, a.y, b.x, b.y};
}

#define BAR() do { asm volatile("s_waitcnt lgkmcnt(0)" ::: "memory"); __builtin_amdgcn_s_barrier(); __builtin_amdgcn_sched_barrier(0); } while (0)
#define WAITV(n) asm volatile("s_waitcnt vmcnt(" #n ")" ::: "memory")
constexpr int G1_AB = 32768, G1_BB = 28672, G1_BOFF = 2 * G1_AB, NT1 = D_ / 64;
__global__ __launch_bounds__(512, 2) void k4_gemm1(const u16* __restrict__ A, const u16* __restrict__ Wg, const u16* __restrict__ Wu, u16* __restrict__ act) {
    __shared__ __attribute__((aligned(16))) char smem[2 * G1_AB + 3 * G1_BB];
    LAS char* lds = (LAS char*)smem;
    const int tid = threadIdx.x, lane = tid & 63, wv = __builtin_amdgcn_readfirstlane(tid >> 6);
    const int bid = blockIdx.x, xcd = bid & 7, loc = bid >> 3;
    const int mt = (xcd & 1) * 4 + (loc & 3), nt = (xcd >> 1) * 8 + (loc >> 2);
    const int row0 = mt * 256, col0 = nt * 112;
    const int grp = wv >> 2, w4 = wv & 3;
    const char* src[8];
    size_t kadv;
    if (grp == 0) {
#pragma unroll
        for (int j = 0; j < 8; ++j) {
            const int pc = w4 + 4 * j, r = pc * 8 + (lane >> 3), cp = lane & 7, cc = cp ^ (r & 7);
            src[j] = (const char*)(A + (size_t)(row0 + r) * D_ + cc * 8);
        }
        kadv = 128;
    } else {
#pragma unroll
        for (int j = 0; j < 8; ++j) {
            int pc = w4 + 4 * j; if (pc >= 28) pc = 0;
            const int ch = pc * 64 + lane, blk = ch >> 3, wi = ch & 7, q = wi >> 1, hf = wi & 1;
            const int kb = blk / 14, pos = blk % 14, nb = pos ^ ((kb >> 1) & 1), k = 4 * kb + q;
            src[j] = nb < 7 ? (const char*)(Wg + (size_t)k * FF + col0 + nb * 16 + hf * 8)
                            : (const char*)(Wu + (size_t)k * FF + col0 + (nb - 7) * 16 + (hf ^ 1) * 8);
        }
        kadv = (size_t)64 * FF * 2;
    }
    auto stageA = [&](int slot) {
        LAS char* b = lds + slot * G1_AB + w4 * 1024;
#pragma unroll
        for (int j = 0; j < 8; ++j) { glds16(src[j], b + j * 4096); src[j] += kadv; }
    };
    auto stageB = [&](int slot) {
        LAS char* b = lds + G1_BOFF + slot * G1_BB + w4 * 1024;
#pragma unroll
        for (int j = 0; j < 7; ++j) { glds16(src[j], b + j * 4096); src[j] += kadv; }
    };
    const int fr = lane & 15, fq = lane >> 4;
    const int aoff0 = (w4 * 64 + fr) * 128 + ((fq ^ (fr & 7)) << 4);
    const int aoff1 = (w4 * 64 + fr) * 128 + (((4 + fq) ^ (fr & 7)) << 4);
    int bo[7];
#pragma unroll
    for (int n = 0; n < 7; ++n) {
        const int j = grp * 7 + n, isup = (fr >> 1) & 1, blk = isup * 7 + (j >> 1);
        bo[n] = G1_BOFF + (2 * fq * 14 + (blk ^ (fq & 1))) * 128 + (fr >> 2) * 32 + (((j & 1) ^ isup) << 4) + (fr & 1) * 8;
    }
    f32x4 acc[4][7];
#pragma unroll
    for (int m = 0; m < 4; ++m)
#pragma unroll
        for (int n = 0; n < 7; ++n) acc[m][n] = f32x4{0.f, 0.f, 0.f, 0.f};
    bf16x8 af[2][4], bfr_[2][7];
    auto ldfr = [&](int aslot, int bslot) {
        LAS char* b = lds + bslot * G1_BB;
        LAS char* a = lds + aslot * G1_AB;
#pragma unroll
        for (int ks = 0; ks < 2; ++ks) {
#pragma unroll
            for (int n = 0; n < 7; ++n) {
                LAS char* pb = b + bo[n] + ks * (8 * 14 * 128);
                bfr_[ks][n] = cat8(trread(pb), trread(pb + 14 * 128));
            }
#pragma unroll
            for (int m = 0; m < 4; ++m) af[ks][m] = *(LAS bf16x8*)(a + (ks ? aoff1 : aoff0) + m * 2048);
        }
    };
    auto mma = [&]() {
        __builtin_amdgcn_s_setprio(1);
#pragma unroll
        for (int ks = 0; ks < 2; ++ks)
#pragma unroll
            for (int n = 0; n < 7; ++n)
#pragma unroll
                for (int m = 0; m < 4; ++m) acc[m][n] = __builtin_amdgcn_mfma_f32_16x16x32_bf16(bfr_[ks][n], af[ks][m], acc[m][n], 0, 0, 0);
        __builtin_amdgcn_s_setprio(0);
    };
    if (grp == 0) { stageA(0); WAITV(0); }
    else { stageB(0); stageB(1); WAITV(7); }
    BAR();
    if (grp == 0) {
        int bs = 0;
        for (int t = 0; t < NT1; ++t) {
            if (t + 1 < NT1) stageA((t + 1) & 1);
            ldfr(t & 1, bs);
            BAR();
            mma();
            WAITV(0);
            BAR();
            bs = bs == 2 ? 0 : bs + 1;
        }
    } else {
        BAR();
        int bs = 0;
        for (int t = 0; t < NT1; ++t) {
            const int b2 = bs == 0 ? 2 : bs - 1;
            if (t + 2 < NT1) stageB(b2);
            ldfr(t & 1, bs);
            if (t + 2 < NT1) { WAITV(7); } else { WAITV(0); }
            BAR();
            mma();
            if (t + 1 < NT1) BAR();
            bs = bs == 2 ? 0 : bs + 1;
        }
    }
    {
        char* ob = (char*)act + ((size_t)((row0 >> 4) + w4 * 4) * (FF / 8) + (col0 >> 3) + grp * 7) * 256 + fr * 16;
        auto grp4 = [&](f32x4& t0, f32x4& t1, f32x4& t2, f32x4& t3, size_t o0, size_t o1, size_t o2, size_t o3) {
            float gp[4], up[4], gq[4], uq[4];
#pragma unroll
            for (int r = 0; r < 4; ++r) {
                auto x = __builtin_amdgcn_permlane32_swap(__float_as_uint(t0[r]), __float_as_uint(t2[r]), false, false);
                gp[r] = __uint_as_float(x[0]); up[r] = __uint_as_float(x[1]);
                auto y = __builtin_amdgcn_permlane32_swap(__float_as_uint(t1[r]), __float_as_uint(t3[r]), false, false);
                gq[r] = __uint_as_float(y[0]); uq[r] = __uint_as_float(y[1]);
            }
            const f2 pa = geglu2(f2{gp[0], gp[1]}, f2{up[0], up[1]}), pb = geglu2(f2{gp[2], gp[3]}, f2{up[2], up[3]});
            const f2 qa = geglu2(f2{gq[0], gq[1]}, f2{uq[0], uq[1]}), qb = geglu2(f2{gq[2], gq[3]}, f2{uq[2], uq[3]});
            const u32 p0 = pack2(pa.x, pa.y), p1 = pack2(pb.x, pb.y), q0 = pack2(qa.x, qa.y), q1 = pack2(qb.x, qb.y);
            auto s0 = __builtin_amdgcn_permlane16_swap(p0, q0, false, false);
            auto s1 = __builtin_amdgcn_permlane16_swap(p1, q1, false, false);
            const size_t o = fq == 0 ? o0 : fq == 1 ? o1 : fq == 2 ? o2 : o3;
            *(u32x4*)(ob + o) = u32x4{s0[0], s1[0], s0[1], s1[1]};
        };
        constexpr size_t MS = (size_t)(FF / 8) * 256;
#pragma unroll
        for (int m = 0; m < 4; ++m)
            grp4(acc[m][0], acc[m][1], acc[m][2], acc[m][3], m * MS, m * MS + 256, m * MS + 512, m * MS + 768);
#pragma unroll
        for (int n = 4; n < 7; ++n)
            grp4(acc[0][n], acc[1][n], acc[2][n], acc[3][n], n * 256, MS + n * 256, 2 * MS + n * 256, 3 * MS + n * 256);
    }
}

constexpr int G2_U = 32768, NU2 = FF / 2 / 64;
__global__ __launch_bounds__(512, 2) void k5_gemm2(const u16* __restrict__ A, const u16* __restrict__ Wd, const u16* __restrict__ h,
                                                   u16* __restrict__ out, f32x4* __restrict__ part, u32* __restrict__ flags) {
    __shared__ __attribute__((aligned(16))) char smem[4 * G2_U];
    LAS char* lds = (LAS char*)smem;
    const int tid = threadIdx.x, lane = tid & 63, wv = __builtin_amdgcn_readfirstlane(tid >> 6);
    const int bid = blockIdx.x, xcd = bid & 7, loc = bid >> 3;
    const int mt = (xcd >> 1) * 4 + (loc & 3), nt = (xcd & 1) * 4 + ((loc >> 2) & 3), ksp = loc >> 4;
    const int row0 = mt * 128, col0 = nt * 128, kbase = ksp * (FF / 2);
    const int grp = wv >> 2, w4 = wv & 3;
    const char* src[8];
#pragma unroll
    for (int j = 0; j < 4; ++j) {
        const int pc = w4 + 4 * j, c = pc >> 1, r = (pc & 1) * 64 + lane;
        src[j] = (const char*)A + ((size_t)((row0 + r) >> 4) * (FF / 8) + (kbase >> 3) + c) * 256 + (r & 15) * 16;
    }
#pragma unroll
    for (int j = 0; j < 4; ++j) {
        const int pc = w4 + 4 * j, ch = pc * 64 + lane, blk = ch >> 3, wi = ch & 7, q = wi >> 1, hf = wi & 1;
        const int kb = blk >> 3, pos = blk & 7, nb = pos ^ ((kb >> 1) & 1), k = 4 * kb + q;
        src[4 + j] = (const char*)(Wd + (size_t)(kbase + k) * D_ + col0 + nb * 16 + hf * 8);
    }
    auto stage = [&](int u) {
        LAS char* b = lds + (u & 3) * G2_U + w4 * 1024;
        const size_t ao = (size_t)u * 8 * 256, bo = (size_t)u * 64 * D_ * 2;
#pragma unroll
        for (int j = 0; j < 4; ++j) glds16(src[j] + ao, b + j * 4096);
#pragma unroll
        for (int j = 0; j < 4; ++j) glds16(src[4 + j] + bo, b + 16384 + j * 4096);
    };
    const int fr = lane & 15, fq = lane >> 4, wr = w4 >> 1, wc = w4 & 1;
    const int aoff = fq * 2048 + (wr * 64 + fr) * 16;
    const int bl = 16384 + (2 * fq * 8) * 128 + (fr >> 2) * 32 + (fr & 3) * 8 + wc * 4 * 128;
    const int sgn = (fq & 1) * 128;
    const int bo0 = bl + sgn, bo1 = bl - sgn;
    f32x4 acc[4][4];
#pragma unroll
    for (int m = 0; m < 4; ++m)
#pragma unroll
        for (int n = 0; n < 4; ++n) acc[m][n] = f32x4{0.f, 0.f, 0.f, 0.f};
    bf16x8 af[2][4], bfr_[2][4];
    auto ldfr = [&](int u) {
        LAS char* b = lds + (u & 3) * G2_U;
#pragma unroll
        for (int ks = 0; ks < 2; ++ks) {
#pragma unroll
            for (int n = 0; n < 4; ++n) {
                LAS char* pb = b + ((n & 1) ? bo1 : bo0) + n * 128 + ks * (8 * 8 * 128);
                bfr_[ks][n] = cat8(trread(pb), trread(pb + 8 * 128));
            }
#pragma unroll
            for (int m = 0; m < 4; ++m) af[ks][m] = *(LAS bf16x8*)(b + aoff + ks * 8192 + m * 256);
        }
    };
    auto mma = [&]() {
        __builtin_amdgcn_s_setprio(1);
#pragma unroll
        for (int ks = 0; ks < 2; ++ks)
#pragma unroll
            for (int n = 0; n < 4; ++n)
#pragma unroll
                for (int m = 0; m < 4; ++m) acc[m][n] = __builtin_amdgcn_mfma_f32_16x16x32_bf16(bfr_[ks][n], af[ks][m], acc[m][n], 0, 0, 0);
        __builtin_amdgcn_s_setprio(0);
    };
    if (grp == 0) { stage(0); stage(2); WAITV(8); }
    else { stage(1); }
    BAR();
    if (grp == 0) {
        for (int u = 0; u < NU2; u += 2) {
            if (u + 3 < NU2) stage(u + 3);
            ldfr(u);
            if (u + 3 < NU2) { WAITV(8); } else { WAITV(0); }
            BAR();
            mma();
            BAR();
        }
    } else {
        WAITV(0);
        BAR();
        for (int u = 1; u < NU2; u += 2) {
            if (u + 3 < NU2) stage(u + 3);
            ldfr(u);
            if (u + 3 < NU2) { WAITV(8); } else { WAITV(0); }
            BAR();
            mma();
            if (u + 1 < NU2) BAR();
        }
    }
    __syncthreads();
    LAS f32x4* ex = (LAS f32x4*)lds + w4 * 16 * 64 + lane;
    if (grp == 1) {
#pragma unroll
        for (int m = 0; m < 4; ++m)
#pragma unroll
            for (int n = 0; n < 4; ++n) ex[(m * 4 + n) * 64] = acc[m][n];
    }
    __syncthreads();
    const int tile = mt * 8 + nt;
    if (grp == 0) {
#pragma unroll
        for (int m = 0; m < 4; ++m)
#pragma unroll
            for (int n = 0; n < 4; ++n) acc[m][n] += ex[(m * 4 + n) * 64];
        if (wr != ksp) {
            __amdgpu_buffer_rsrc_t rs = __builtin_amdgcn_make_buffer_rsrc((void*)part, 0, 0x7fffffff, 0x00020000);
            const int off = (((tile * 2 + ksp) * 2 + wc) * 16 * 64 + lane) * 16;
#pragma unroll
            for (int m = 0; m < 4; ++m)
#pragma unroll
                for (int n = 0; n < 4; ++n)
                    __builtin_amdgcn_raw_buffer_store_b128(__builtin_bit_cast(u32x4, acc[m][n]), rs, off + (m * 4 + n) * 1024, 0, 16);
            WAITV(0);
        }
    }
    __syncthreads();
    GAS u32* fl = (GAS u32*)flags;
    if (tid == 0) __hip_atomic_store(fl + (tile * 2 + ksp) * 16, 1u, __ATOMIC_RELAXED, __HIP_MEMORY_SCOPE_AGENT);
    if (grp == 0 && wr == ksp) {
        u32x2 hv[4][4];
        const size_t ro = (size_t)(row0 + wr * 64 + fr) * D_ + col0 + wc * 64 + fq * 4;
#pragma unroll
        for (int m = 0; m < 4; ++m)
#pragma unroll
            for (int n = 0; n < 4; ++n) hv[m][n] = *(const u32x2*)(h + ro + (size_t)m * 16 * D_ + n * 16);
        unsigned spins = 0;
        while (__builtin_amdgcn_readfirstlane(__hip_atomic_load(fl + (tile * 2 + (ksp ^ 1)) * 16, __ATOMIC_RELAXED, __HIP_MEMORY_SCOPE_AGENT)) == 0u) {
            __builtin_amdgcn_s_sleep(1);
            if (++spins > (1u << 22)) break;
        }
        __amdgpu_buffer_rsrc_t rs = __builtin_amdgcn_make_buffer_rsrc((void*)part, 0, 0x7fffffff, 0x00020000);
        const int toff = (((tile * 2 + (ksp ^ 1)) * 2 + wc) * 16 * 64 + lane) * 16;
        f32x4 o[4][4];
#pragma unroll
        for (int m = 0; m < 4; ++m)
#pragma unroll
            for (int n = 0; n < 4; ++n) o[m][n] = __builtin_bit_cast(f32x4, __builtin_amdgcn_raw_buffer_load_b128(rs, toff + (m * 4 + n) * 1024, 0, 16));
#pragma unroll
        for (int m = 0; m < 4; ++m)
#pragma unroll
            for (int n = 0; n < 4; ++n) {
                const f32x4 sum = acc[m][n] + o[m][n];
                float r[4];
#pragma unroll
                for (int j = 0; j < 4; ++j) {
                    const u32 hw = hv[m][n][j >> 1];
                    r[j] = bf2f((j & 1) ? (hw >> 16) : (hw & 0xffffu)) + bfr(sum[j]);
                }
                *(u32x2*)(out + ro + (size_t)m * 16 * D_ + n * 16) = u32x2{pack2(r[0], r[1]), pack2(r[2], r[3])};
            }
    }
}

extern "C" void kernel_launch(void* const* d_in, const int* in_sizes, int n_in, void* d_out, int out_size, void* d_ws, size_t ws_size,
                              hipStream_t stream) {
    const u16* x = (const u16*)d_in[0];
    const u16* wr = (const u16*)d_in[1];
    const u16* wi = (const u16*)d_in[2];
    const u16* wg = (const u16*)d_in[4];
    const u16* wu = (const u16*)d_in[5];
    const u16* wd = (const u16*)d_in[6];
    u16* out = (u16*)d_out;
    char* ws = (char*)d_ws;
    f2* P = (f2*)(ws);
    u32* Wt = (u32*)(ws + (8u << 20));
    u16* h = (u16*)(ws + (13u << 20));
    u16* ln2 = (u16*)(ws + (17u << 20));
    u16* act = (u16*)(ws + (21u << 20));
    f32x4* part = (f32x4*)(ws + (36u << 20));
    u32* flags = (u32*)(ws + (70u << 20));
    k1_ln_fft<<<256 + 288, 512, 0, stream>>>(x, P, wr, wi, Wt, flags);
    k2_col<<<256, 256, 0, stream>>>(P, Wt, wg, wu, wd);
    k3_ifft_ln<<<256, 512, 0, stream>>>(P, x, h, ln2);
    k4_gemm1<<<256, 512, 0, stream>>>(ln2, wg, wu, act);
    k5_gemm2<<<256, 512, 0, stream>>>(act, wd, h, out, part, flags);
}
```

```cpp
#include <hip/hip_runtime.h>
#include <hip/hip_bf16.h>

typedef unsigned short u16;
typedef unsigned int u32;
typedef float f2 __attribute__((ext_vector_type(2)));
typedef float f32x4 __attribute__((ext_vector_type(4)));
typedef short bf16x8 __attribute__((ext_vector_type(8)));
typedef short s16x4 __attribute__((ext_vector_type(4)));
typedef unsigned u32x4 __attribute__((ext_vector_type(4)));
typedef unsigned u32x2 __attribute__((ext_vector_type(2)));
#define LAS __attribute__((address_space(3)))
#define GAS __attribute__((address_space(1)))

constexpr int S_ = 2048, D_ = 1024, DF = 513, FF = 3584;

__device__ __forceinline__ float bf2f(u32 h) { return __uint_as_float(h << 16); }
__device__ __forceinline__ u32 f2bf(float f) { __bf16 b = (__bf16)f; return (u32)__builtin_bit_cast(u16, b); }
__device__ __forceinline__ float bfr(float f) { return bf2f(f2bf(f)); }
typedef __bf16 bf16x2_t __attribute__((ext_vector_type(2)));
__device__ __forceinline__ u32 pack2(float lo, float hi) { return __builtin_bit_cast(u32, __builtin_convertvector(f2{lo, hi}, bf16x2_t)); }

__device__ __forceinline__ f2 add_ib(f2 a, f2 b) {
    f2 r; asm("v_pk_add_f32 %0, %1, %2 op_sel:[0,1] op_sel_hi:[1,0] neg_lo:[0,1]" : "=v"(r) : "v"(a), "v"(b)); return r; }
__device__ __forceinline__ f2 sub_ib(f2 a, f2 b) {
    f2 r; asm("v_pk_add_f32 %0, %1, %2 op_sel:[0,1] op_sel_hi:[1,0] neg_hi:[0,1]" : "=v"(r) : "v"(a), "v"(b)); return r; }
__device__ __forceinline__ f2 add_cj(f2 a, f2 b) {
    f2 r; asm("v_pk_add_f32 %0, %1, %2 neg_hi:[0,1]" : "=v"(r) : "v"(a), "v"(b)); return r; }
__device__ __forceinline__ f2 sub_cj(f2 a, f2 b) {
    f2 r; asm("v_pk_add_f32 %0, %1, %2 neg_lo:[0,1]" : "=v"(r) : "v"(a), "v"(b)); return r; }
__device__ __forceinline__ f2 cj_sub_ib(f2 a, f2 b) {
    f2 r; asm("v_pk_add_f32 %0, %1, %2 op_sel:[0,1] op_sel_hi:[1,0] neg_hi:[1,0]" : "=v"(r) : "v"(a), "v"(b)); return r; }
__device__ __forceinline__ f2 cmul(f2 a, f2 w) {
    f2 t, r;
    asm("v_pk_mul_f32 %0, %1, %2 op_sel:[0,0] op_sel_hi:[0,1]" : "=v"(t) : "v"(a), "v"(w));
    asm("v_pk_fma_f32 %0, %1, %2, %3 op_sel:[1,1,0] op_sel_hi:[1,0,1] neg_lo:[0,1,0]" : "=v"(r) : "v"(a), "v"(w), "v"(t));
    return r; }
__device__ __forceinline__ f2 cmulc(f2 a, f2 w) {
    f2 t, r;
    asm("v_pk_mul_f32 %0, %1, %2 op_sel:[0,0] op_sel_hi:[0,1] neg_hi:[0,1]" : "=v"(t) : "v"(a), "v"(w));
    asm("v_pk_fma_f32 %0, %1, %2, %3 op_sel:[1,1,0] op_sel_hi:[1,0,1]" : "=v"(r) : "v"(a), "v"(w), "v"(t));
    return r; }
template <int D> __device__ __forceinline__ f2 twm(f2 a, f2 w) { return D > 0 ? cmulc(a, w) : cmul(a, w); }
template <int D> __device__ __forceinline__ f2 add_di(f2 a, f2 b) { return D > 0 ? add_ib(a, b) : sub_ib(a, b); }
template <int D> __device__ __forceinline__ f2 sub_di(f2 a, f2 b) { return D > 0 ? sub_ib(a, b) : add_ib(a, b); }

template <int D> __device__ __forceinline__ void dft4(f2& x0, f2& x1, f2& x2, f2& x3) {
    const f2 t0 = x0 + x2, t1 = x0 - x2, t2 = x1 + x3, d = x1 - x3;
    x0 = t0 + t2; x2 = t0 - t2; x1 = add_di<D>(t1, d); x3 = sub_di<D>(t1, d);
}
template <int D> __device__ __forceinline__ void dft8(f2* x) {
    dft4<D>(x[0], x[2], x[4], x[6]);
    dft4<D>(x[1], x[3], x[5], x[7]);
    const float c = 0.70710678118654752440f;
    const f2 o1 = add_di<D>(x[3], x[3]) * c;
    const f2 o3 = sub_di<D>(x[7], x[7]) * c;
    const f2 e0 = x[0], e1 = x[2], e2 = x[4], e3 = x[6], o0 = x[1], o2 = x[5];
    x[0] = e0 + o0; x[4] = e0 - o0;
    x[1] = e1 + o1; x[5] = e1 - o1;
    x[2] = add_di<D>(e2, o2); x[6] = sub_di<D>(e2, o2);
    x[3] = e3 - o3; x[7] = e3 + o3;
}
template <int D> __device__ __forceinline__ void dft16(f2* x) {
    f2 e[8], o[8];
#pragma unroll
    for (int i = 0; i < 8; ++i) { e[i] = x[2 * i]; o[i] = x[2 * i + 1]; }
    dft8<D>(e); dft8<D>(o);
    const float c1 = 0.92387953251128675613f, s1 = 0.38268343236508977173f, c2 = 0.70710678118654752440f;
    o[1] = twm<D>(o[1], f2{c1, -s1});
    o[2] = add_di<D>(o[2], o[2]) * c2;
    o[3] = twm<D>(o[3], f2{s1, -c1});
    o[5] = twm<D>(o[5], f2{-s1, -c1});
    o[6] = sub_di<D>(o[6], o[6]) * c2;
    o[7] = twm<D>(o[7], f2{-c1, -s1});
#pragma unroll
    for (int i = 0; i < 8; ++i) {
        if (i == 4) { x[4] = add_di<D>(e[4], o[4]); x[12] = sub_di<D>(e[4], o[4]); }
        else if (i == 6) { x[6] = e[6] - o[6]; x[14] = e[6] + o[6]; }
        else { x[i] = e[i] + o[i]; x[i + 8] = e[i] - o[i]; }
    }
}
struct TwTab { float v[4096]; };
constexpr double c_sin(double x) { double t = x, r = x; for (int i = 1; i < 12; ++i) { t *= -x * x / ((2 * i) * (2 * i + 1)); r += t; } return r; }
constexpr double c_cos(double x) { double t = 1, r = 1; for (int i = 1; i < 12; ++i) { t *= -x * x / ((2 * i - 1) * (2 * i)); r += t; } return r; }
constexpr TwTab make_tw() {
    TwTab t{};
    const double step = 6.283185307179586476925286766559 / 2048.0;
    for (int k = 0; k < 2048; ++k) {
        const int q = k >> 9, r = k & 511;
        double c = 0, s = 0;
        if (r <= 256) { c = c_cos(r * step); s = c_sin(r * step); } else { c = c_sin((512 - r) * step); s = c_cos((512 - r) * step); }
        double cc = q == 0 ? c : q == 1 ? -s : q == 2 ? -c : s;
        double ss = q == 0 ? s : q == 1 ? c : q == 2 ? -s : -c;
        t.v[2 * k] = (float)cc; t.v[2 * k + 1] = (float)(-ss);
    }
    return t;
}
__device__ const TwTab TW = make_tw();
__device__ __forceinline__ f2 twl(int k) { return *(const f2*)(TW.v + 2 * k); }
template <int N> __device__ __forceinline__ void twchain(f2* w) {
#pragma unroll
    for (int k = 2; k < N; ++k) w[k] = (k & 1) ? cmul(w[k - 1], w[1]) : cmul(w[k / 2], w[k / 2]);
}
__device__ __forceinline__ float wave_sum(float v) {
#pragma unroll
    for (int o = 32; o >= 1; o >>= 1) v += __shfl_xor(v, o);
    return v;
}
__device__ __forceinline__ void ln_row(f2* x) {
    f2 s2 = (x[0] + x[1]) + (x[2] + x[3]) + ((x[4] + x[5]) + (x[6] + x[7]));
    const float mean = wave_sum(s2.x + s2.y) * (1.0f / 1024.0f);
    f2 q2 = f2{0.f, 0.f};
#pragma unroll
    for (int m = 0; m < 8; ++m) { const f2 d = x[m] - mean; q2 += d * d; }
    const float var = wave_sum(q2.x + q2.y) * (1.0f / 1024.0f);
    const float mb = bfr(mean);
    const float rden = 1.0f / bfr(sqrtf(bfr(bfr(var) + 1e-5f)));
#pragma unroll
    for (int m = 0; m < 8; ++m) {
        const f2 d = x[m] - mb;
        const u32 db = pack2(d.x, d.y);
        const f2 q = f2{bf2f(db & 0xffffu), __uint_as_float(db & 0xffff0000u)} * rden;
        const u32 qb = pack2(q.x, q.y);
        x[m] = f2{bf2f(qb & 0xffffu), __uint_as_float(qb & 0xffff0000u)};
    }
}

__device__ __forceinline__ void glds16(const void* g, LAS void* l) {
    unsigned sv; const unsigned lb = (unsigned)__builtin_amdgcn_readfirstlane((unsigned)(size_t)l);
    asm volatile("s_mov_b32 %0, m0\n\ts_mov_b32 m0, %2\n\ts_nop 0\n\tglobal_load_lds_dwordx4 %1, off\n\ts_mov_b32 m0, %0" : "=&s"(sv) : "v"(g), "s"(lb) : "memory");
}
template <int NTH> __device__ __forceinline__ void prefetch_w(const u16* w, int gtid, LAS char* scr) {
#pragma unroll
    for (int i = 0; i < (458752 + NTH - 1) / NTH; ++i) {
        const int ch = gtid + i * NTH;
        if (458752 % NTH == 0 || __builtin_amdgcn_readfirstlane(ch) < 458752) glds16((const char*)w + (size_t)ch * 16, scr);
    }
}
constexpr int RS = 580;
__global__ __launch_bounds__(512) void k1_ln_fft(const u16* __restrict__ x, f2* __restrict__ P, const u16* __restrict__ wr,
                                                 const u16* __restrict__ wi, u32* __restrict__ Wt, u32* __restrict__ flags, const u16* __restrict__ wpf) {
    __shared__ __attribute__((aligned(16))) f2 sm[8 * RS + 1024];
    const int tid = threadIdx.x, lane = tid & 63, wv = tid >> 6;
    if (blockIdx.x >= 256) {
        const int b = blockIdx.x - 256;
        if (b == 0 && tid < 256) flags[tid * 16] = 0u;
        u32* tile = (u32*)sm;
        const int k0 = (b & 31) * 64, c0 = (b >> 5) * 64;
#pragma unroll
        for (int i = 0; i < 8; ++i) {
            const int r = wv + 8 * i, c = c0 + lane;
            u32 v = 0;
            if (c < DF) v = (u32)wr[(k0 + r) * DF + c] | ((u32)wi[(k0 + r) * DF + c] << 16);
            tile[r * 65 + lane] = v;
        }
        __syncthreads();
#pragma unroll
        for (int i = 0; i < 8; ++i) {
            const int c = wv + 8 * i;
            if (c0 + c < DF) Wt[(c0 + c) * S_ + k0 + lane] = tile[lane * 65 + c];
        }
        return;
    }
    const int s = blockIdx.x * 8 + wv;
    const u32* xr = (const u32*)x + (size_t)s * 512;
    f2 v[8], w1[8], w2[8];
    u32 raw[8];
#pragma unroll
    for (int m = 0; m < 8; ++m) raw[m] = xr[lane + 64 * m];
    w1[1] = twl(4 * lane); w2[1] = twl(32 * (lane & 7));
#pragma unroll
    for (int m = 0; m < 8; ++m) v[m] = f2{bf2f(raw[m] & 0xffffu), __uint_as_float(raw[m] & 0xffff0000u)};
    ln_row(v);
    prefetch_w<131072>(wpf, blockIdx.x * 512 + tid, (LAS char*)(sm + 8 * RS) + wv * 1024);
    f2* R = sm + wv * RS;
    dft8<-1>(v);
    twchain<8>(w1);
    R[lane] = v[0];
#pragma unroll
    for (int k = 1; k < 8; ++k) R[k * 72 + lane] = cmul(v[k], w1[k]);
    __builtin_amdgcn_wave_barrier();
    {
        const int k1 = lane >> 3, b = lane & 7;
#pragma unroll
        for (int a = 0; a < 8; ++a) v[a] = R[k1 * 72 + 8 * a + b];
        dft8<-1>(v);
        twchain<8>(w2);
        __builtin_amdgcn_wave_barrier();
        R[k1 * 72 + b * 9] = v[0];
#pragma unroll
        for (int k = 1; k < 8; ++k) R[k1 * 72 + b * 9 + k] = cmul(v[k], w2[k]);
    }
    __builtin_amdgcn_wave_barrier();
    {
        const int k1 = lane & 7, k2 = lane >> 3;
#pragma unroll
        for (int b = 0; b < 8; ++b) v[b] = R[k1 * 72 + 9 * b + k2];
        dft8<-1>(v);
    }
    __builtin_amdgcn_wave_barrier();
#pragma unroll
    for (int k = 0; k < 8; ++k) R[lane + 64 * k] = v[k];
    __syncthreads();
    const int s0 = blockIdx.x * 8;
#pragma unroll
    for (int i = 0; i < 4; ++i) {
        const int it = tid + 512 * i, r = it & 7, p = it >> 3;
        const int ca = p, cb = p ? 512 - p : 256;
        f2 a = sm[r * RS + ca], b = sm[r * RS + cb];
        *(f32x4*)(P + ((size_t)p * S_ + s0 + r) * 2) = f32x4{a.x, a.y, b.x, b.y};
    }
    asm volatile("s_waitcnt vmcnt(0)" ::: "memory");
}

constexpr int CS = 2192;
__device__ __forceinline__ f2 unpk(u32 u) { return f2{bf2f(u & 0xffffu), __uint_as_float(u & 0xffff0000u)}; }
__device__ __forceinline__ void pair_gen(f2& za, f2& zb, u32 fa, u32 fb, f2 tw, f2 twi) {
    const f2 E = add_cj(za, zb), Dd = sub_cj(za, zb);
    const f2 wO = cmul(Dd, twi);
    const f2 Xa = E + wO, Xbc = E - wO;
    const f2 Ya = cmul(Xa, unpk(fa)), Ybc = cmulc(Xbc, unpk(fb));
    const f2 E2 = Ya + Ybc, O2 = cmulc(Ya - Ybc, tw);
    za = add_ib(E2, O2);
    zb = cj_sub_ib(E2, O2);
}
__device__ __forceinline__ void pair_zero(f2& za, f2& zb, u32 f0a, u32 f0b, u32 f5a, u32 f5b) {
    const f2 E = add_cj(za, zb), Dd = sub_cj(za, zb);
    const f2 X0 = sub_ib(E, Dd), X5 = add_ib(E, Dd);
    const f2 s0 = (cmul(X0, unpk(f0a)) + cmulc(X0, unpk(f0b))) * 0.5f;
    const f2 s5 = (cmul(X5, unpk(f5a)) + cmulc(X5, unpk(f5b))) * 0.5f;
    const f2 E2 = s0 + s5, O2 = s0 - s5;
    za = add_ib(E2, O2);
    zb = cj_sub_ib(E2, O2);
}
__global__ __launch_bounds__(256) void k2_col(f2* __restrict__ P, const u32* __restrict__ Wt, const u16* __restrict__ wu) {
    __shared__ __attribute__((aligned(16))) f2 sm[2 * CS + 512];
    const int p = blockIdx.x, tid = threadIdx.x;
    const int c = tid & 1, t = tid >> 1, b = t & 7, k1 = t >> 3;
    f2* base = P + (size_t)p * 4096;
    f2 v[16], w1[16], w2[16];
#pragma unroll
    for (int m = 0; m < 16; ++m) v[m] = base[(128 * m + t) * 2 + c];
    w1[1] = twl(t); w2[1] = twl(16 * b);
    const int T = tid, tt = T & 127, cz = T >> 7;
    const int ka = p ? T : tt, kb = p ? ((256 - T) & 255) : (tt ? 256 - tt : 128);
    const int sa = p ? 0 : cz, sb = p ? 1 : cz;
    const int colA = p ? p : cz * 256, colB = p ? 512 - p : cz * 256;
    const f2 tw = twl(2 * colA);
    u32 wa[8], wb[8], wc[8], wd[8];
#pragma unroll
    for (int j = 0; j < 8; ++j) { wa[j] = Wt[colA * S_ + ka + 256 * j]; wb[j] = Wt[colB * S_ + kb + 256 * j]; }
    if (p == 0) {
#pragma unroll
        for (int j = 0; j < 8; ++j) { wc[j] = Wt[512 * S_ + ka + 256 * j]; wd[j] = Wt[512 * S_ + kb + 256 * j]; }
    }
    twchain<16>(w1); twchain<16>(w2);
    dft16<-1>(v);
    f2* C = sm + c * CS;
    C[t] = v[0];
#pragma unroll
    for (int k = 1; k < 16; ++k) C[k * 136 + t] = cmul(v[k], w1[k]);
    __syncthreads();
    prefetch_w<65536>(wu, p * 256 + tid, (LAS char*)(sm + 2 * CS) + (tid >> 6) * 1024);
#pragma unroll
    for (int a = 0; a < 16; ++a) v[a] = C[k1 * 136 + 8 * a + b];
    dft16<-1>(v);
    __syncthreads();
    C[b * 258 + k1] = v[0];
#pragma unroll
    for (int k = 1; k < 16; ++k) C[b * 258 + k1 + 16 * k] = cmul(v[k], w2[k]);
    __syncthreads();
    f2* A = v; f2* B = v + 8;
    f2* pa = sm + sa * CS + ka; f2* pb = sm + sb * CS + kb;
#pragma unroll
    for (int j = 0; j < 8; ++j) { A[j] = pa[j * 258]; B[j] = pb[j * 258]; }
    dft8<-1>(A); dft8<-1>(B);
    const f2 twi = f2{tw.y, -tw.x};
    if (p) {
        if (T == 0) {
            f2 t0 = B[0]; u32 w0 = wb[0];
#pragma unroll
            for (int j = 0; j < 7; ++j) { B[j] = B[j + 1]; wb[j] = wb[j + 1]; }
            B[7] = t0; wb[7] = w0;
        }
#pragma unroll
        for (int j = 0; j < 8; ++j) pair_gen(A[j], B[7 - j], wa[j], wb[7 - j], tw, twi);
        if (T == 0) {
            f2 t7 = B[7];
#pragma unroll
            for (int j = 7; j > 0; --j) B[j] = B[j - 1];
            B[0] = t7;
        }
    } else if (tt) {
        if (cz) {
#pragma unroll
            for (int j = 0; j < 8; ++j) pair_gen(A[j], B[7 - j], wa[j], wb[7 - j], tw, twi);
        } else {
#pragma unroll
            for (int j = 0; j < 8; ++j) pair_zero(A[j], B[7 - j], wa[j], wb[7 - j], wc[j], wd[7 - j]);
        }
    } else {
        f2 An[8];
#pragma unroll
        for (int j = 0; j < 8; ++j) {
            f2 za = A[j], zb = A[(8 - j) & 7];
            if (cz) pair_gen(za, zb, wa[j], wa[(8 - j) & 7], tw, twi);
            else pair_zero(za, zb, wa[j], wa[(8 - j) & 7], wc[j], wc[(8 - j) & 7]);
            An[j] = za;
        }
#pragma unroll
        for (int j = 0; j < 8; ++j) A[j] = An[j];
#pragma unroll
        for (int j = 0; j < 4; ++j) {
            if (cz) pair_gen(B[j], B[7 - j], wb[j], wb[7 - j], tw, twi);
            else pair_zero(B[j], B[7 - j], wb[j], wb[7 - j], wd[j], wd[7 - j]);
        }
    }
    dft8<1>(A); dft8<1>(B);
    __syncthreads();
#pragma unroll
    for (int j = 0; j < 8; ++j) { pa[j * 258] = A[j]; pb[j * 258] = B[j]; }
    __syncthreads();
    v[0] = C[b * 258 + k1];
#pragma unroll
    for (int k = 1; k < 16; ++k) v[k] = cmulc(C[b * 258 + k1 + 16 * k], w2[k]);
    dft16<1>(v);
    __syncthreads();
#pragma unroll
    for (int a = 0; a < 16; ++a) C[k1 * 136 + 8 * a + b] = v[a];
    __syncthreads();
    v[0] = C[t];
#pragma unroll
    for (int k = 1; k < 16; ++k) v[k] = cmulc(C[k * 136 + t], w1[k]);
    dft16<1>(v);
#pragma unroll
    for (int m = 0; m < 16; ++m) base[(128 * m + t) * 2 + c] = v[m];
    asm volatile("s_waitcnt vmcnt(0)" ::: "memory");
}

__global__ __launch_bounds__(512) void k3_ifft_ln(const f2* __restrict__ P, const u16* __restrict__ x, u16* __restrict__ h, u16* __restrict__ ln2, const u16* __restrict__ wpf) {
    __shared__ __attribute__((aligned(16))) f2 sm[8 * RS + 1024];
    const int tid = threadIdx.x, lane = tid & 63, wv = tid >> 6;
    const int s0 = blockIdx.x * 8, s = s0 + wv;
    const u32* xr = (const u32*)x + (size_t)s * 512;
    f32x4 q[4];
#pragma unroll
    for (int i = 0; i < 4; ++i) {
        const int it = tid + 512 * i, r = it & 7, p = it >> 3;
        q[i] = *(const f32x4*)(P + ((size_t)p * S_ + s0 + r) * 2);
    }
    u32 xraw[8];
#pragma unroll
    for (int m = 0; m < 8; ++m) xraw[m] = xr[lane + 64 * m];
    f2 v[8], w1[8], w2[8];
    w1[1] = twl(4 * lane); w2[1] = twl(32 * (lane & 7));
    twchain<8>(w1); twchain<8>(w2);
#pragma unroll
    for (int i = 0; i < 4; ++i) {
        const int it = tid + 512 * i, r = it & 7, p = it >> 3;
        const int ca = p, cb = p ? 512 - p : 256;
        sm[r * RS + ca] = f2{q[i][0], q[i][1]};
        sm[r * RS + cb] = f2{q[i][2], q[i][3]};
    }
    __syncthreads();
    prefetch_w<131072>(wpf, blockIdx.x * 512 + tid, (LAS char*)(sm + 8 * RS) + wv * 1024);
    f2* R = sm + wv * RS;
#pragma unroll
    for (int k = 0; k < 8; ++k) v[k] = R[lane + 64 * k];
    dft8<1>(v);
    __builtin_amdgcn_wave_barrier();
    {
        const int k1 = lane & 7, k2 = lane >> 3;
#pragma unroll
        for (int b = 0; b < 8; ++b) R[k1 * 72 + 9 * b + k2] = v[b];
    }
    __builtin_amdgcn_wave_barrier();
    {
        const int k1 = lane >> 3, b = lane & 7;
        v[0] = R[k1 * 72 + b * 9];
#pragma unroll
        for (int k = 1; k < 8; ++k) v[k] = cmulc(R[k1 * 72 + b * 9 + k], w2[k]);
        dft8<1>(v);
        __builtin_amdgcn_wave_barrier();
#pragma unroll
        for (int a = 0; a < 8; ++a) R[k1 * 72 + 8 * a + b] = v[a];
    }
    __builtin_amdgcn_wave_barrier();
    v[0] = R[lane];
#pragma unroll
    for (int k = 1; k < 8; ++k) v[k] = cmulc(R[k * 72 + lane], w1[k]);
    dft8<1>(v);
    u32* hr = (u32*)h + (size_t)s * 512;
    u32* lr = (u32*)ln2 + (size_t)s * 512;
    const float NRM = 1.0f / 4194304.0f;
#pragma unroll
    for (int m = 0; m < 8; ++m) {
        const f2 y = v[m] * NRM;
        const f2 hs = unpk(xraw[m]) + unpk(pack2(y.x, y.y));
        const u32 hb = pack2(hs.x, hs.y);
        hr[lane + 64 * m] = hb;
        v[m] = unpk(hb);
    }
    ln_row(v);
#pragma unroll
    for (int m = 0; m < 8; ++m) lr[lane + 64 * m] = pack2(v[m].x, v[m].y);
    asm volatile("s_waitcnt vmcnt(0)" ::: "memory");
}

typedef short v4i16_t __attribute__((ext_vector_type(4)));
__device__ __forceinline__ s16x4 trread(LAS char* p) { return __builtin_bit_cast(s16x4, __builtin_amdgcn_ds_read_tr16_b64_v4i16((LAS v4i16_t*)p)); }
__device__ __forceinline__ bf16x8 cat8(s16x4 a, s16x4 b) { return bf16x8{a[0], a[1], a[2], a[3], b[0], b[1], b[2], b[3]}; }
#define WAITV0() asm volatile("s_waitcnt vmcnt(0)" ::: "memory")

__device__ __forceinline__ float gelu_tanh(float x) {
    const float k0 = 0.7978845608028654f, k1 = 0.044715f;
    const float u = k0 * (x + k1 * x * x * x);
    const float e = __expf(-2.0f * u);
    return x / (1.0f + e);
}

__device__ __forceinline__ f2 geglu2(f2 g, f2 u) {
    const float c0 = -2.0f * 0.7978845608028654f * 1.4426950408889634f, c1 = c0 * 0.044715f;
    const f2 z = g * g;
    const f2 t = g * (z * c1 + c0);
    f2 e; e.x = __builtin_amdgcn_exp2f(t.x); e.y = __builtin_amdgcn_exp2f(t.y);
    const f2 d = e + 1.0f;
    f2 r; r.x = __builtin_amdgcn_rcpf(d.x); r.y = __builtin_amdgcn_rcpf(d.y);
    return (g * r) * u;
}
__device__ __forceinline__ f32x4 geglu4(f32x4 g, f32x4 u) {
    const f2 a = geglu2(f2{g[0], g[1]}, f2{u[0], u[1]}), b = geglu2(f2{g[2], g[3]}, f2{u[2], u[3]});
    return f32x4{a.x, a.y, b.x, b.y};
}

#define BAR() do { asm volatile("s_waitcnt lgkmcnt(0)" ::: "memory"); __builtin_amdgcn_s_barrier(); __builtin_amdgcn_sched_barrier(0); } while (0)
#define WAITV(n) asm volatile("s_waitcnt vmcnt(" #n ")" ::: "memory")
constexpr int G1_AB = 32768, G1_BB = 28672, G1_BOFF = 2 * G1_AB, NT1 = D_ / 64;
__global__ __launch_bounds__(512, 2) void k4_gemm1(const u16* __restrict__ A, const u16* __restrict__ Wg, const u16* __restrict__ Wu, u16* __restrict__ act) {
    __shared__ __attribute__((aligned(16))) char smem[2 * G1_AB + 3 * G1_BB];
    LAS char* lds = (LAS char*)smem;
    const int tid = threadIdx.x, lane = tid & 63, wv = __builtin_amdgcn_readfirstlane(tid >> 6);
    const int bid = blockIdx.x, xcd = bid & 7, loc = bid >> 3;
    const int mt = (xcd & 1) * 4 + (loc & 3), nt = (xcd >> 1) * 8 + (loc >> 2);
    const int row0 = mt * 256, col0 = nt * 112;
    const int grp = wv >> 2, w4 = wv & 3;
    const char* src[8];
    size_t kadv;
    if (grp == 0) {
#pragma unroll
        for (int j = 0; j < 8; ++j) {
            const int pc = w4 + 4 * j, r = pc * 8 + (lane >> 3), cp = lane & 7, cc = cp ^ (r & 7);
            src[j] = (const char*)(A + (size_t)(row0 + r) * D_ + cc * 8);
        }
        kadv = 128;
    } else {
#pragma unroll
        for (int j = 0; j < 8; ++j) {
            int pc = w4 + 4 * j; if (pc >= 28) pc = 0;
            const int ch = pc * 64 + lane, blk = ch >> 3, wi = ch & 7, q = wi >> 1, hf = wi & 1;
            const int kb = blk / 14, pos = blk % 14, nb = pos ^ ((kb >> 1) & 1), k = 4 * kb + q;
            src[j] = nb < 7 ? (const char*)(Wg + (size_t)k * FF + col0 + nb * 16 + hf * 8)
                            : (const char*)(Wu + (size_t)k * FF + col0 + (nb - 7) * 16 + (hf ^ 1) * 8);
        }
        kadv = (size_t)64 * FF * 2;
    }
    auto stageA = [&](int slot) {
        LAS char* b = lds + slot * G1_AB + w4 * 1024;
#pragma unroll
        for (int j = 0; j < 8; ++j) { glds16(src[j], b + j * 4096); src[j] += kadv; }
    };
    auto stageB = [&](int slot) {
        LAS char* b = lds + G1_BOFF + slot * G1_BB + w4 * 1024;
#pragma unroll
        for (int j = 0; j < 7; ++j) { glds16(src[j], b + j * 4096); src[j] += kadv; }
    };
    const int fr = lane & 15, fq = lane >> 4;
    const int aoff0 = (w4 * 64 + fr) * 128 + ((fq ^ (fr & 7)) << 4);
    const int aoff1 = (w4 * 64 + fr) * 128 + (((4 + fq) ^ (fr & 7)) << 4);
    int bo[7];
#pragma unroll
    for (int n = 0; n < 7; ++n) {
        const int j = grp * 7 + n, isup = (fr >> 1) & 1, blk = isup * 7 + (j >> 1);
        bo[n] = G1_BOFF + (2 * fq * 14 + (blk ^ (fq & 1))) * 128 + (fr >> 2) * 32 + (((j & 1) ^ isup) << 4) + (fr & 1) * 8;
    }
    f32x4 acc[4][7];
#pragma unroll
    for (int m = 0; m < 4; ++m)
#pragma unroll
        for (int n = 0; n < 7; ++n) acc[m][n] = f32x4{0.f, 0.f, 0.f, 0.f};
    bf16x8 af[2][4], bfr_[2][7];
    auto ldfr = [&](int aslot, int bslot) {
        LAS char* b = lds + bslot * G1_BB;
        LAS char* a = lds + aslot * G1_AB;
#pragma unroll
        for (int ks = 0; ks < 2; ++ks) {
#pragma unroll
            for (int n = 0; n < 7; ++n) {
                LAS char* pb = b + bo[n] + ks * (8 * 14 * 128);
                bfr_[ks][n] = cat8(trread(pb), trread(pb + 14 * 128));
            }
#pragma unroll
            for (int m = 0; m < 4; ++m) af[ks][m] = *(LAS bf16x8*)(a + (ks ? aoff1 : aoff0) + m * 2048);
        }
    };
    auto mma = [&]() {
        __builtin_amdgcn_s_setprio(1);
#pragma unroll
        for (int ks = 0; ks < 2; ++ks)
#pragma unroll
            for (int n = 0; n < 7; ++n)
#pragma unroll
                for (int m = 0; m < 4; ++m) acc[m][n] = __builtin_amdgcn_mfma_f32_16x16x32_bf16(bfr_[ks][n], af[ks][m], acc[m][n], 0, 0, 0);
        __builtin_amdgcn_s_setprio(0);
    };
    if (grp == 0) { stageA(0); WAITV(0); }
    else { stageB(0); stageB(1); WAITV(7); }
    BAR();
    if (grp == 0) {
        int bs = 0;
        for (int t = 0; t < NT1; ++t) {
            ldfr(t & 1, bs);
            if (t + 1 < NT1) stageA((t + 1) & 1);
            BAR();
            mma();
            WAITV(0);
            BAR();
            bs = bs == 2 ? 0 : bs + 1;
        }
    } else {
        BAR();
        int bs = 0;
        for (int t = 0; t < NT1; ++t) {
            const int b2 = bs == 0 ? 2 : bs - 1;
            ldfr(t & 1, bs);
            if (t + 2 < NT1) stageB(b2);
            if (t + 2 < NT1) { WAITV(7); } else { WAITV(0); }
            BAR();
            mma();
            if (t + 1 < NT1) BAR();
            bs = bs == 2 ? 0 : bs + 1;
        }
    }
    {
        char* ob = (char*)act + ((size_t)((row0 >> 4) + w4 * 4) * (FF / 8) + (col0 >> 3) + grp * 7) * 256 + fr * 16;
        auto grp4 = [&](f32x4& t0, f32x4& t1, f32x4& t2, f32x4& t3, size_t o0, size_t o1, size_t o2, size_t o3) {
            float gp[4], up[4], gq[4], uq[4];
#pragma unroll
            for (int r = 0; r < 4; ++r) {
                auto x = __builtin_amdgcn_permlane32_swap(__float_as_uint(t0[r]), __float_as_uint(t2[r]), false, false);
                gp[r] = __uint_as_float(x[0]); up[r] = __uint_as_float(x[1]);
                auto y = __builtin_amdgcn_permlane32_swap(__float_as_uint(t1[r]), __float_as_uint(t3[r]), false, false);
                gq[r] = __uint_as_float(y[0]); uq[r] = __uint_as_float(y[1]);
            }
            const f2 pa = geglu2(f2{gp[0], gp[1]}, f2{up[0], up[1]}), pb = geglu2(f2{gp[2], gp[3]}, f2{up[2], up[3]});
            const f2 qa = geglu2(f2{gq[0], gq[1]}, f2{uq[0], uq[1]}), qb = geglu2(f2{gq[2], gq[3]}, f2{uq[2], uq[3]});
            const u32 p0 = pack2(pa.x, pa.y), p1 = pack2(pb.x, pb.y), q0 = pack2(qa.x, qa.y), q1 = pack2(qb.x, qb.y);
            auto s0 = __builtin_amdgcn_permlane16_swap(p0, q0, false, false);
            auto s1 = __builtin_amdgcn_permlane16_swap(p1, q1, false, false);
            const size_t o = fq == 0 ? o0 : fq == 1 ? o1 : fq == 2 ? o2 : o3;
            *(u32x4*)(ob + o) = u32x4{s0[0], s1[0], s0[1], s1[1]};
        };
        constexpr size_t MS = (size_t)(FF / 8) * 256;
#pragma unroll
        for (int m = 0; m < 4; ++m)
            grp4(acc[m][0], acc[m][1], acc[m][2], acc[m][3], m * MS, m * MS + 256, m * MS + 512, m * MS + 768);
#pragma unroll
        for (int n = 4; n < 7; ++n)
            grp4(acc[0][n], acc[1][n], acc[2][n], acc[3][n], n * 256, MS + n * 256, 2 * MS + n * 256, 3 * MS + n * 256);
    }
}

constexpr int G2_U = 32768, NU2 = FF / 2 / 64;
__global__ __launch_bounds__(512, 2) void k5_gemm2(const u16* __restrict__ A, const u16* __restrict__ Wd, const u16* __restrict__ h,
                                                   u16* __restrict__ out, f32x4* __restrict__ part, u32* __restrict__ flags) {
    __shared__ __attribute__((aligned(16))) char smem[4 * G2_U];
    LAS char* lds = (LAS char*)smem;
    const int tid = threadIdx.x, lane = tid & 63, wv = __builtin_amdgcn_readfirstlane(tid >> 6);
    const int bid = blockIdx.x, xcd = bid & 7, loc = bid >> 3;
    const int mt = (xcd >> 1) * 4 + (loc & 3), nt = (xcd & 1) * 4 + ((loc >> 2) & 3), ksp = loc >> 4;
    const int row0 = mt * 128, col0 = nt * 128, kbase = ksp * (FF / 2);
    const int grp = wv >> 2, w4 = wv & 3;
    const char* src[8];
#pragma unroll
    for (int j = 0; j < 4; ++j) {
        const int pc = w4 + 4 * j, c = pc >> 1, r = (pc & 1) * 64 + lane;
        src[j] = (const char*)A + ((size_t)((row0 + r) >> 4) * (FF / 8) + (kbase >> 3) + c) * 256 + (r & 15) * 16;
    }
#pragma unroll
    for (int j = 0; j < 4; ++j) {
        const int pc = w4 + 4 * j, ch = pc * 64 + lane, blk = ch >> 3, wi = ch & 7, q = wi >> 1, hf = wi & 1;
        const int kb = blk >> 3, pos = blk & 7, nb = pos ^ ((kb >> 1) & 1), k = 4 * kb + q;
        src[4 + j] = (const char*)(Wd + (size_t)(kbase + k) * D_ + col0 + nb * 16 + hf * 8);
    }
    auto stage = [&](int u) {
        LAS char* b = lds + (u & 3) * G2_U + w4 * 1024;
        const size_t ao = (size_t)u * 8 * 256, bo = (size_t)u * 64 * D_ * 2;
#pragma unroll
        for (int j = 0; j < 4; ++j) glds16(src[j] + ao, b + j * 4096);
#pragma unroll
        for (int j = 0; j < 4; ++j) glds16(src[4 + j] + bo, b + 16384 + j * 4096);
    };
    const int fr = lane & 15, fq = lane >> 4, wr = w4 >> 1, wc = w4 & 1;
    const int aoff = fq * 2048 + (wr * 64 + fr) * 16;
    const int bl = 16384 + (2 * fq * 8) * 128 + (fr >> 2) * 32 + (fr & 3) * 8 + wc * 4 * 128;
    const int sgn = (fq & 1) * 128;
    const int bo0 = bl + sgn, bo1 = bl - sgn;
    f32x4 acc[4][4];
#pragma unroll
    for (int m = 0; m < 4; ++m)
#pragma unroll
        for (int n = 0; n < 4; ++n) acc[m][n] = f32x4{0.f, 0.f, 0.f, 0.f};
    bf16x8 af[2][4], bfr_[2][4];
    auto ldfr = [&](int u) {
        LAS char* b = lds + (u & 3) * G2_U;
#pragma unroll
        for (int ks = 0; ks < 2; ++ks) {
#pragma unroll
            for (int n = 0; n < 4; ++n) {
                LAS char* pb = b + ((n & 1) ? bo1 : bo0) + n * 128 + ks * (8 * 8 * 128);
                bfr_[ks][n] = cat8(trread(pb), trread(pb + 8 * 128));
            }
#pragma unroll
            for (int m = 0; m < 4; ++m) af[ks][m] = *(LAS bf16x8*)(b + aoff + ks * 8192 + m * 256);
        }
    };
    auto mma = [&]() {
        __builtin_amdgcn_s_setprio(1);
#pragma unroll
        for (int ks = 0; ks < 2; ++ks)
#pragma unroll
            for (int n = 0; n < 4; ++n)
#pragma unroll
                for (int m = 0; m < 4; ++m) acc[m][n] = __builtin_amdgcn_mfma_f32_16x16x32_bf16(bfr_[ks][n], af[ks][m], acc[m][n], 0, 0, 0);
        __builtin_amdgcn_s_setprio(0);
    };
    if (grp == 0) { stage(0); stage(2); WAITV(8); }
    else { stage(1); }
    BAR();
    if (grp == 0) {
        for (int u = 0; u < NU2; u += 2) {
            if (u + 3 < NU2) stage(u + 3);
            ldfr(u);
            if (u + 3 < NU2) { WAITV(8); } else { WAITV(0); }
            BAR();
            mma();
            BAR();
        }
    } else {
        WAITV(0);
        BAR();
        for (int u = 1; u < NU2; u += 2) {
            if (u + 3 < NU2) stage(u + 3);
            ldfr(u);
            if (u + 3 < NU2) { WAITV(8); } else { WAITV(0); }
            BAR();
            mma();
            if (u + 1 < NU2) BAR();
        }
    }
    const int tile = mt * 8 + nt;
    GAS u32* fl = (GAS u32*)flags;
    __amdgpu_buffer_rsrc_t rs = __builtin_amdgcn_make_buffer_rsrc((void*)part, 0, 0x7fffffff, 0x00020000);
    LAS f32x4* ex = (LAS f32x4*)lds + wc * 16 * 64 + lane;
    if (wr != ksp) {
        const int off = ((((tile * 2 + ksp) * 2 + grp) * 2 + wc) * 16 * 64 + lane) * 16;
#pragma unroll
        for (int m = 0; m < 4; ++m)
#pragma unroll
            for (int n = 0; n < 4; ++n)
                __builtin_amdgcn_raw_buffer_store_b128(__builtin_bit_cast(u32x4, acc[m][n]), rs, off + (m * 4 + n) * 1024, 0, 16);
        WAITV(0);
        if (lane == 0) __hip_atomic_fetch_add(fl + (tile * 2 + ksp) * 16, 1u, __ATOMIC_RELAXED, __HIP_MEMORY_SCOPE_AGENT);
    } else if (grp == 1) {
#pragma unroll
        for (int m = 0; m < 4; ++m)
#pragma unroll
            for (int n = 0; n < 4; ++n) ex[(m * 4 + n) * 64] = acc[m][n];
    }
    __syncthreads();
    const bool fin = (grp == 0) && (wr == ksp);
    u32x2 hv[4][4];
    const size_t ro = (size_t)(row0 + wr * 64 + fr) * D_ + col0 + wc * 64 + fq * 4;
    if (fin) {
#pragma unroll
        for (int m = 0; m < 4; ++m)
#pragma unroll
            for (int n = 0; n < 4; ++n) hv[m][n] = *(const u32x2*)(h + ro + (size_t)m * 16 * D_ + n * 16);
#pragma unroll
        for (int m = 0; m < 4; ++m)
#pragma unroll
            for (int n = 0; n < 4; ++n) acc[m][n] += ex[(m * 4 + n) * 64];
        if (wc == 0) {
            unsigned spins = 0;
            while (__builtin_amdgcn_readfirstlane(__hip_atomic_load(fl + (tile * 2 + (ksp ^ 1)) * 16, __ATOMIC_RELAXED, __HIP_MEMORY_SCOPE_AGENT)) < 4u) {
                __builtin_amdgcn_s_sleep(1);
                if (++spins > (1u << 22)) break;
            }
        }
    }
    __syncthreads();
    if (fin) {
        const int toff = (((tile * 2 + (ksp ^ 1)) * 2 * 2 + wc) * 16 * 64 + lane) * 16;
        f32x4 o0[4][4], o1[4][4];
#pragma unroll
        for (int m = 0; m < 4; ++m)
#pragma unroll
            for (int n = 0; n < 4; ++n) {
                o0[m][n] = __builtin_bit_cast(f32x4, __builtin_amdgcn_raw_buffer_load_b128(rs, toff + (m * 4 + n) * 1024, 0, 16));
                o1[m][n] = __builtin_bit_cast(f32x4, __builtin_amdgcn_raw_buffer_load_b128(rs, toff + 2 * 16 * 1024 + (m * 4 + n) * 1024, 0, 16));
            }
#pragma unroll
        for (int m = 0; m < 4; ++m)
#pragma unroll
            for (int n = 0; n < 4; ++n) {
                const f32x4 sum = acc[m][n] + (o0[m][n] + o1[m][n]);
                float r[4];
#pragma unroll
                for (int j = 0; j < 4; ++j) {
                    const u32 hw = hv[m][n][j >> 1];
                    r[j] = bf2f((j & 1) ? (hw >> 16) : (hw & 0xffffu)) + bfr(sum[j]);
                }
                *(u32x2*)(out + ro + (size_t)m * 16 * D_ + n * 16) = u32x2{pack2(r[0], r[1]), pack2(r[2], r[3])};
            }
    }
}

extern "C" void kernel_launch(void* const* d_in, const int* in_sizes, int n_in, void* d_out, int out_size, void* d_ws, size_t ws_size,
                              hipStream_t stream) {
    const u16* x = (const u16*)d_in[0];
    const u16* wr = (const u16*)d_in[1];
    const u16* wi = (const u16*)d_in[2];
    const u16* wg = (const u16*)d_in[4];
    const u16* wu = (const u16*)d_in[5];
    const u16* wd = (const u16*)d_in[6];
    u16* out = (u16*)d_out;
    char* ws = (char*)d_ws;
    f2* P = (f2*)(ws);
    u32* Wt = (u32*)(ws + (8u << 20));
    u16* h = (u16*)(ws + (13u << 20));
    u16* ln2 = (u16*)(ws + (17u << 20));
    u16* act = (u16*)(ws + (21u << 20));
    f32x4* part = (f32x4*)(ws + (36u << 20));
    u32* flags = (u32*)(ws + (70u << 20));
    k1_ln_fft<<<256 + 288, 512, 0, stream>>>(x, P, wr, wi, Wt, flags, wg);
    k2_col<<<256, 256, 0, stream>>>(P, Wt, wu);
    k3_ifft_ln<<<256, 512, 0, stream>>>(P, x, h, ln2, wd);
    k4_gemm1<<<256, 512, 0, stream>>>(ln2, wg, wu, act);
    k5_gemm2<<<256, 512, 0, stream>>>(act, wd, h, out, part, flags);
}
```

```cpp
#include <hip/hip_runtime.h>
#include <hip/hip_bf16.h>

typedef unsigned short u16;
typedef unsigned int u32;
typedef float f2 __attribute__((ext_vector_type(2)));
typedef float f32x4 __attribute__((ext_vector_type(4)));
typedef short bf16x8 __attribute__((ext_vector_type(8)));
typedef short s16x4 __attribute__((ext_vector_type(4)));
typedef unsigned u32x4 __attribute__((ext_vector_type(4)));
typedef unsigned u32x2 __attribute__((ext_vector_type(2)));
#define LAS __attribute__((address_space(3)))
#define GAS __attribute__((address_space(1)))

constexpr int S_ = 2048, D_ = 1024, DF = 513, FF = 3584;

__device__ __forceinline__ float bf2f(u32 h) { return __uint_as_float(h << 16); }
__device__ __forceinline__ u32 f2bf(float f) { __bf16 b = (__bf16)f; return (u32)__builtin_bit_cast(u16, b); }
__device__ __forceinline__ float bfr(float f) { return bf2f(f2bf(f)); }
typedef __bf16 bf16x2_t __attribute__((ext_vector_type(2)));
__device__ __forceinline__ u32 pack2(float lo, float hi) { return __builtin_bit_cast(u32, __builtin_convertvector(f2{lo, hi}, bf16x2_t)); }

__device__ __forceinline__ f2 add_ib(f2 a, f2 b) {
    f2 r; asm("v_pk_add_f32 %0, %1, %2 op_sel:[0,1] op_sel_hi:[1,0] neg_lo:[0,1]" : "=v"(r) : "v"(a), "v"(b)); return r; }
__device__ __forceinline__ f2 sub_ib(f2 a, f2 b) {
    f2 r; asm("v_pk_add_f32 %0, %1, %2 op_sel:[0,1] op_sel_hi:[1,0] neg_hi:[0,1]" : "=v"(r) : "v"(a), "v"(b)); return r; }
__device__ __forceinline__ f2 add_cj(f2 a, f2 b) {
    f2 r; asm("v_pk_add_f32 %0, %1, %2 neg_hi:[0,1]" : "=v"(r) : "v"(a), "v"(b)); return r; }
__device__ __forceinline__ f2 sub_cj(f2 a, f2 b) {
    f2 r; asm("v_pk_add_f32 %0, %1, %2 neg_lo:[0,1]" : "=v"(r) : "v"(a), "v"(b)); return r; }
__device__ __forceinline__ f2 cj_sub_ib(f2 a, f2 b) {
    f2 r; asm("v_pk_add_f32 %0, %1, %2 op_sel:[0,1] op_sel_hi:[1,0] neg_hi:[1,0]" : "=v"(r) : "v"(a), "v"(b)); return r; }
__device__ __forceinline__ f2 cmul(f2 a, f2 w) {
    f2 t, r;
    asm("v_pk_mul_f32 %0, %1, %2 op_sel:[0,0] op_sel_hi:[0,1]" : "=v"(t) : "v"(a), "v"(w));
    asm("v_pk_fma_f32 %0, %1, %2, %3 op_sel:[1,1,0] op_sel_hi:[1,0,1] neg_lo:[0,1,0]" : "=v"(r) : "v"(a), "v"(w), "v"(t));
    return r; }
__device__ __forceinline__ f2 cmulc(f2 a, f2 w) {
    f2 t, r;
    asm("v_pk_mul_f32 %0, %1, %2 op_sel:[0,0] op_sel_hi:[0,1] neg_hi:[0,1]" : "=v"(t) : "v"(a), "v"(w));
    asm("v_pk_fma_f32 %0, %1, %2, %3 op_sel:[1,1,0] op_sel_hi:[1,0,1]" : "=v"(r) : "v"(a), "v"(w), "v"(t));
    return r; }
template <int D> __device__ __forceinline__ f2 twm(f2 a, f2 w) { return D > 0 ? cmulc(a, w) : cmul(a, w); }
template <int D> __device__ __forceinline__ f2 add_di(f2 a, f2 b) { return D > 0 ? add_ib(a, b) : sub_ib(a, b); }
template <int D> __device__ __forceinline__ f2 sub_di(f2 a, f2 b) { return D > 0 ? sub_ib(a, b) : add_ib(a, b); }

template <int D> __device__ __forceinline__ void dft4(f2& x0, f2& x1, f2& x2, f2& x3) {
    const f2 t0 = x0 + x2, t1 = x0 - x2, t2 = x1 + x3, d = x1 - x3;
    x0 = t0 + t2; x2 = t0 - t2; x1 = add_di<D>(t1, d); x3 = sub_di<D>(t1, d);
}
template <int D> __device__ __forceinline__ void dft8(f2* x) {
    dft4<D>(x[0], x[2], x[4], x[6]);
    dft4<D>(x[1], x[3], x[5], x[7]);
    const float c = 0.70710678118654752440f;
    const f2 o1 = add_di<D>(x[3], x[3]) * c;
    const f2 o3 = sub_di<D>(x[7], x[7]) * c;
    const f2 e0 = x[0], e1 = x[2], e2 = x[4], e3 = x[6], o0 = x[1], o2 = x[5];
    x[0] = e0 + o0; x[4] = e0 - o0;
    x[1] = e1 + o1; x[5] = e1 - o1;
    x[2] = add_di<D>(e2, o2); x[6] = sub_di<D>(e2, o2);
    x[3] = e3 - o3; x[7] = e3 + o3;
}
template <int D> __device__ __forceinline__ void dft16(f2* x) {
    f2 e[8], o[8];
#pragma unroll
    for (int i = 0; i < 8; ++i) { e[i] = x[2 * i]; o[i] = x[2 * i + 1]; }
    dft8<D>(e); dft8<D>(o);
    const float c1 = 0.92387953251128675613f, s1 = 0.38268343236508977173f, c2 = 0.70710678118654752440f;
    o[1] = twm<D>(o[1], f2{c1, -s1});
    o[2] = add_di<D>(o[2], o[2]) * c2;
    o[3] = twm<D>(o[3], f2{s1, -c1});
    o[5] = twm<D>(o[5], f2{-s1, -c1});
    o[6] = sub_di<D>(o[6], o[6]) * c2;
    o[7] = twm<D>(o[7], f2{-c1, -s1});
#pragma unroll
    for (int i = 0; i < 8; ++i) {
        if (i == 4) { x[4] = add_di<D>(e[4], o[4]); x[12] = sub_di<D>(e[4], o[4]); }
        else if (i == 6) { x[6] = e[6] - o[6]; x[14] = e[6] + o[6]; }
        else { x[i] = e[i] + o[i]; x[i + 8] = e[i] - o[i]; }
    }
}
struct TwTab { float v[4096]; };
constexpr double c_sin(double x) { double t = x, r = x; for (int i = 1; i < 12; ++i) { t *= -x * x / ((2 * i) * (2 * i + 1)); r += t; } return r; }
constexpr double c_cos(double x) { double t = 1, r = 1; for (int i = 1; i < 12; ++i) { t *= -x * x / ((2 * i - 1) * (2 * i)); r += t; } return r; }
constexpr TwTab make_tw() {
    TwTab t{};
    const double step = 6.283185307179586476925286766559 / 2048.0;
    for (int k = 0; k < 2048; ++k) {
        const int q = k >> 9, r = k & 511;
        double c = 0, s = 0;
        if (r <= 256) { c = c_cos(r * step); s = c_sin(r * step); } else { c = c_sin((512 - r) * step); s = c_cos((512 - r) * step); }
        double cc = q == 0 ? c : q == 1 ? -s : q == 2 ? -c : s;
        double ss = q == 0 ? s : q == 1 ? c : q == 2 ? -s : -c;
        t.v[2 * k] = (float)cc; t.v[2 * k + 1] = (float)(-ss);
    }
    return t;
}
__device__ const TwTab TW = make_tw();
__device__ __forceinline__ f2 twl(int k) { return *(const f2*)(TW.v + 2 * k); }
template <int N> __device__ __forceinline__ void twchain(f2* w) {
#pragma unroll
    for (int k = 2; k < N; ++k) w[k] = (k & 1) ? cmul(w[k - 1], w[1]) : cmul(w[k / 2], w[k / 2]);
}
__device__ __forceinline__ float wave_sum(float v) {
#pragma unroll
    for (int o = 32; o >= 1; o >>= 1) v += __shfl_xor(v, o);
    return v;
}
__device__ __forceinline__ void ln_row(f2* x) {
    f2 s2 = (x[0] + x[1]) + (x[2] + x[3]) + ((x[4] + x[5]) + (x[6] + x[7]));
    const float mean = wave_sum(s2.x + s2.y) * (1.0f / 1024.0f);
    f2 q2 = f2{0.f, 0.f};
#pragma unroll
    for (int m = 0; m < 8; ++m) { const f2 d = x[m] - mean; q2 += d * d; }
    const float var = wave_sum(q2.x + q2.y) * (1.0f / 1024.0f);
    const float mb = bfr(mean);
    const float rden = 1.0f / bfr(sqrtf(bfr(bfr(var) + 1e-5f)));
#pragma unroll
    for (int m = 0; m < 8; ++m) {
        const f2 d = x[m] - mb;
        const u32 db = pack2(d.x, d.y);
        const f2 q = f2{bf2f(db & 0xffffu), __uint_as_float(db & 0xffff0000u)} * rden;
        const u32 qb = pack2(q.x, q.y);
        x[m] = f2{bf2f(qb & 0xffffu), __uint_as_float(qb & 0xffff0000u)};
    }
}

__device__ __forceinline__ void glds16(const void* g, LAS void* l) {
    unsigned sv; const unsigned lb = (unsigned)__builtin_amdgcn_readfirstlane((unsigned)(size_t)l);
    asm volatile("s_mov_b32 %0, m0\n\ts_mov_b32 m0, %2\n\ts_nop 0\n\tglobal_load_lds_dwordx4 %1, off\n\ts_mov_b32 m0, %0" : "=&s"(sv) : "v"(g), "s"(lb) : "memory");
}
template <int NTH> __device__ __forceinline__ void prefetch_w(const u16* w, int gtid, LAS char* scr) {
#pragma unroll
    for (int i = 0; i < (458752 + NTH - 1) / NTH; ++i) {
        const int ch = gtid + i * NTH;
        if (458752 % NTH == 0 || __builtin_amdgcn_readfirstlane(ch) < 458752) glds16((const char*)w + (size_t)ch * 16, scr);
    }
}
constexpr int RS = 580;
__global__ __launch_bounds__(512) void k1_ln_fft(const u16* __restrict__ x, f2* __restrict__ P, const u16* __restrict__ wr,
                                                 const u16* __restrict__ wi, u32* __restrict__ Wt, u32* __restrict__ flags, const u16* __restrict__ wpf) {
    __shared__ __attribute__((aligned(16))) f2 sm[8 * RS + 1024];
    const int tid = threadIdx.x, lane = tid & 63, wv = tid >> 6;
    if (blockIdx.x >= 256) {
        const int b = blockIdx.x - 256;
        if (b == 0 && tid < 256) flags[tid * 16] = 0u;
        u32* tile = (u32*)sm;
        const int k0 = (b & 31) * 64, c0 = (b >> 5) * 64;
#pragma unroll
        for (int i = 0; i < 8; ++i) {
            const int r = wv + 8 * i, c = c0 + lane;
            u32 v = 0;
            if (c < DF) v = (u32)wr[(k0 + r) * DF + c] | ((u32)wi[(k0 + r) * DF + c] << 16);
            tile[r * 65 + lane] = v;
        }
        __syncthreads();
#pragma unroll
        for (int i = 0; i < 8; ++i) {
            const int c = wv + 8 * i;
            if (c0 + c < DF) Wt[(c0 + c) * S_ + k0 + lane] = tile[lane * 65 + c];
        }
        return;
    }
    const int s = blockIdx.x * 8 + wv;
    const u32* xr = (const u32*)x + (size_t)s * 512;
    f2 v[8], w1[8], w2[8];
    u32 raw[8];
#pragma unroll
    for (int m = 0; m < 8; ++m) raw[m] = xr[lane + 64 * m];
    w1[1] = twl(4 * lane); w2[1] = twl(32 * (lane & 7));
#pragma unroll
    for (int m = 0; m < 8; ++m) v[m] = f2{bf2f(raw[m] & 0xffffu), __uint_as_float(raw[m] & 0xffff0000u)};
    ln_row(v);
    prefetch_w<131072>(wpf, blockIdx.x * 512 + tid, (LAS char*)(sm + 8 * RS) + wv * 1024);
    f2* R = sm + wv * RS;
    dft8<-1>(v);
    twchain<8>(w1);
    R[lane] = v[0];
#pragma unroll
    for (int k = 1; k < 8; ++k) R[k * 72 + lane] = cmul(v[k], w1[k]);
    __builtin_amdgcn_wave_barrier();
    {
        const int k1 = lane >> 3, b = lane & 7;
#pragma unroll
        for (int a = 0; a < 8; ++a) v[a] = R[k1 * 72 + 8 * a + b];
        dft8<-1>(v);
        twchain<8>(w2);
        __builtin_amdgcn_wave_barrier();
        R[k1 * 72 + b * 9] = v[0];
#pragma unroll
        for (int k = 1; k < 8; ++k) R[k1 * 72 + b * 9 + k] = cmul(v[k], w2[k]);
    }
    __builtin_amdgcn_wave_barrier();
    {
        const int k1 = lane & 7, k2 = lane >> 3;
#pragma unroll
        for (int b = 0; b < 8; ++b) v[b] = R[k1 * 72 + 9 * b + k2];
        dft8<-1>(v);
    }
    __builtin_amdgcn_wave_barrier();
#pragma unroll
    for (int k = 0; k < 8; ++k) R[lane + 64 * k] = v[k];
    __syncthreads();
    const int s0 = blockIdx.x * 8;
#pragma unroll
    for (int i = 0; i < 4; ++i) {
        const int it = tid + 512 * i, r = it & 7, p = it >> 3;
        const int ca = p, cb = p ? 512 - p : 256;
        f2 a = sm[r * RS + ca], b = sm[r * RS + cb];
        *(f32x4*)(P + ((size_t)p * S_ + s0 + r) * 2) = f32x4{a.x, a.y, b.x, b.y};
    }
    asm volatile("s_waitcnt vmcnt(0)" ::: "memory");
}

constexpr int CS = 2192;
__device__ __forceinline__ f2 unpk(u32 u) { return f2{bf2f(u & 0xffffu), __uint_as_float(u & 0xffff0000u)}; }
__device__ __forceinline__ void pair_gen(f2& za, f2& zb, u32 fa, u32 fb, f2 tw, f2 twi) {
    const f2 E = add_cj(za, zb), Dd = sub_cj(za, zb);
    const f2 wO = cmul(Dd, twi);
    const f2 Xa = E + wO, Xbc = E - wO;
    const f2 Ya = cmul(Xa, unpk(fa)), Ybc = cmulc(Xbc, unpk(fb));
    const f2 E2 = Ya + Ybc, O2 = cmulc(Ya - Ybc, tw);
    za = add_ib(E2, O2);
    zb = cj_sub_ib(E2, O2);
}
__device__ __forceinline__ void pair_zero(f2& za, f2& zb, u32 f0a, u32 f0b, u32 f5a, u32 f5b) {
    const f2 E = add_cj(za, zb), Dd = sub_cj(za, zb);
    const f2 X0 = sub_ib(E, Dd), X5 = add_ib(E, Dd);
    const f2 s0 = (cmul(X0, unpk(f0a)) + cmulc(X0, unpk(f0b))) * 0.5f;
    const f2 s5 = (cmul(X5, unpk(f5a)) + cmulc(X5, unpk(f5b))) * 0.5f;
    const f2 E2 = s0 + s5, O2 = s0 - s5;
    za = add_ib(E2, O2);
    zb = cj_sub_ib(E2, O2);
}
__global__ __launch_bounds__(256) void k2_col(f2* __restrict__ P, const u32* __restrict__ Wt, const u16* __restrict__ wu) {
    __shared__ __attribute__((aligned(16))) f2 sm[2 * CS + 512];
    const int p = blockIdx.x, tid = threadIdx.x;
    const int c = tid & 1, t = tid >> 1, b = t & 7, k1 = t >> 3;
    f2* base = P + (size_t)p * 4096;
    f2 v[16], w1[16], w2[16];
#pragma unroll
    for (int m = 0; m < 16; ++m) v[m] = base[(128 * m + t) * 2 + c];
    w1[1] = twl(t); w2[1] = twl(16 * b);
    const int T = tid, tt = T & 127, cz = T >> 7;
    const int ka = p ? T : tt, kb = p ? ((256 - T) & 255) : (tt ? 256 - tt : 128);
    const int sa = p ? 0 : cz, sb = p ? 1 : cz;
    const int colA = p ? p : cz * 256, colB = p ? 512 - p : cz * 256;
    const f2 tw = twl(2 * colA);
    u32 wa[8], wb[8], wc[8], wd[8];
#pragma unroll
    for (int j = 0; j < 8; ++j) { wa[j] = Wt[colA * S_ + ka + 256 * j]; wb[j] = Wt[colB * S_ + kb + 256 * j]; }
    if (p == 0) {
#pragma unroll
        for (int j = 0; j < 8; ++j) { wc[j] = Wt[512 * S_ + ka + 256 * j]; wd[j] = Wt[512 * S_ + kb + 256 * j]; }
    }
    twchain<16>(w1); twchain<16>(w2);
    dft16<-1>(v);
    f2* C = sm + c * CS;
    C[t] = v[0];
#pragma unroll
    for (int k = 1; k < 16; ++k) C[k * 136 + t] = cmul(v[k], w1[k]);
    __syncthreads();
    prefetch_w<65536>(wu, p * 256 + tid, (LAS char*)(sm + 2 * CS) + (tid >> 6) * 1024);
#pragma unroll
    for (int a = 0; a < 16; ++a) v[a] = C[k1 * 136 + 8 * a + b];
    dft16<-1>(v);
    __syncthreads();
    C[b * 258 + k1] = v[0];
#pragma unroll
    for (int k = 1; k < 16; ++k) C[b * 258 + k1 + 16 * k] = cmul(v[k], w2[k]);
    __syncthreads();
    f2* A = v; f2* B = v + 8;
    f2* pa = sm + sa * CS + ka; f2* pb = sm + sb * CS + kb;
#pragma unroll
    for (int j = 0; j < 8; ++j) { A[j] = pa[j * 258]; B[j] = pb[j * 258]; }
    dft8<-1>(A); dft8<-1>(B);
    const f2 twi = f2{tw.y, -tw.x};
    if (p) {
        if (T == 0) {
            f2 t0 = B[0]; u32 w0 = wb[0];
#pragma unroll
            for (int j = 0; j < 7; ++j) { B[j] = B[j + 1]; wb[j] = wb[j + 1]; }
            B[7] = t0; wb[7] = w0;
        }
#pragma unroll
        for (int j = 0; j < 8; ++j) pair_gen(A[j], B[7 - j], wa[j], wb[7 - j], tw, twi);
        if (T == 0) {
            f2 t7 = B[7];
#pragma unroll
            for (int j = 7; j > 0; --j) B[j] = B[j - 1];
            B[0] = t7;
        }
    } else if (tt) {
        if (cz) {
#pragma unroll
            for (int j = 0; j < 8; ++j) pair_gen(A[j], B[7 - j], wa[j], wb[7 - j], tw, twi);
        } else {
#pragma unroll
            for (int j = 0; j < 8; ++j) pair_zero(A[j], B[7 - j], wa[j], wb[7 - j], wc[j], wd[7 - j]);
        }
    } else {
        f2 An[8];
#pragma unroll
        for (int j = 0; j < 8; ++j) {
            f2 za = A[j], zb = A[(8 - j) & 7];
            if (cz) pair_gen(za, zb, wa[j], wa[(8 - j) & 7], tw, twi);
            else pair_zero(za, zb, wa[j], wa[(8 - j) & 7], wc[j], wc[(8 - j) & 7]);
            An[j] = za;
        }
#pragma unroll
        for (int j = 0; j < 8; ++j) A[j] = An[j];
#pragma unroll
        for (int j = 0; j < 4; ++j) {
            if (cz) pair_gen(B[j], B[7 - j], wb[j], wb[7 - j], tw, twi);
            else pair_zero(B[j], B[7 - j], wb[j], wb[7 - j], wd[j], wd[7 - j]);
        }
    }
    dft8<1>(A); dft8<1>(B);
    __syncthreads();
#pragma unroll
    for (int j = 0; j < 8; ++j) { pa[j * 258] = A[j]; pb[j * 258] = B[j]; }
    __syncthreads();
    v[0] = C[b * 258 + k1];
#pragma unroll
    for (int k = 1; k < 16; ++k) v[k] = cmulc(C[b * 258 + k1 + 16 * k], w2[k]);
    dft16<1>(v);
    __syncthreads();
#pragma unroll
    for (int a = 0; a < 16; ++a) C[k1 * 136 + 8 * a + b] = v[a];
    __syncthreads();
    v[0] = C[t];
#pragma unroll
    for (int k = 1; k < 16; ++k) v[k] = cmulc(C[k * 136 + t], w1[k]);
    dft16<1>(v);
#pragma unroll
    for (int m = 0; m < 16; ++m) base[(128 * m + t) * 2 + c] = v[m];
    asm volatile("s_waitcnt vmcnt(0)" ::: "memory");
}

__global__ __launch_bounds__(512) void k3_ifft_ln(const f2* __restrict__ P, const u16* __restrict__ x, u16* __restrict__ h, u16* __restrict__ ln2, const u16* __restrict__ wpf) {
    __shared__ __attribute__((aligned(16))) f2 sm[8 * RS + 1024];
    const int tid = threadIdx.x, lane = tid & 63, wv = tid >> 6;
    const int s0 = blockIdx.x * 8, s = s0 + wv;
    const u32* xr = (const u32*)x + (size_t)s * 512;
    f32x4 q[4];
#pragma unroll
    for (int i = 0; i < 4; ++i) {
        const int it = tid + 512 * i, r = it & 7, p = it >> 3;
        q[i] = *(const f32x4*)(P + ((size_t)p * S_ + s0 + r) * 2);
    }
    u32 xraw[8];
#pragma unroll
    for (int m = 0; m < 8; ++m) xraw[m] = xr[lane + 64 * m];
    f2 v[8], w1[8], w2[8];
    w1[1] = twl(4 * lane); w2[1] = twl(32 * (lane & 7));
    twchain<8>(w1); twchain<8>(w2);
#pragma unroll
    for (int i = 0; i < 4; ++i) {
        const int it = tid + 512 * i, r = it & 7, p = it >> 3;
        const int ca = p, cb = p ? 512 - p : 256;
        sm[r * RS + ca] = f2{q[i][0], q[i][1]};
        sm[r * RS + cb] = f2{q[i][2], q[i][3]};
    }
    __syncthreads();
    prefetch_w<131072>(wpf, blockIdx.x * 512 + tid, (LAS char*)(sm + 8 * RS) + wv * 1024);
    f2* R = sm + wv * RS;
#pragma unroll
    for (int k = 0; k < 8; ++k) v[k] = R[lane + 64 * k];
    dft8<1>(v);
    __builtin_amdgcn_wave_barrier();
    {
        const int k1 = lane & 7, k2 = lane >> 3;
#pragma unroll
        for (int b = 0; b < 8; ++b) R[k1 * 72 + 9 * b + k2] = v[b];
    }
    __builtin_amdgcn_wave_barrier();
    {
        const int k1 = lane >> 3, b = lane & 7;
        v[0] = R[k1 * 72 + b * 9];
#pragma unroll
        for (int k = 1; k < 8; ++k) v[k] = cmulc(R[k1 * 72 + b * 9 + k], w2[k]);
        dft8<1>(v);
        __builtin_amdgcn_wave_barrier();
#pragma unroll
        for (int a = 0; a < 8; ++a) R[k1 * 72 + 8 * a + b] = v[a];
    }
    __builtin_amdgcn_wave_barrier();
    v[0] = R[lane];
#pragma unroll
    for (int k = 1; k < 8; ++k) v[k] = cmulc(R[k * 72 + lane], w1[k]);
    dft8<1>(v);
    u32* hr = (u32*)h + (size_t)s * 512;
    u32* lr = (u32*)ln2 + (size_t)s * 512;
    const float NRM = 1.0f / 4194304.0f;
#pragma unroll
    for (int m = 0; m < 8; ++m) {
        const f2 y = v[m] * NRM;
        const f2 hs = unpk(xraw[m]) + unpk(pack2(y.x, y.y));
        const u32 hb = pack2(hs.x, hs.y);
        hr[lane + 64 * m] = hb;
        v[m] = unpk(hb);
    }
    ln_row(v);
#pragma unroll
    for (int m = 0; m < 8; ++m) lr[lane + 64 * m] = pack2(v[m].x, v[m].y);
    asm volatile("s_waitcnt vmcnt(0)" ::: "memory");
}

typedef short v4i16_t __attribute__((ext_vector_type(4)));
__device__ __forceinline__ s16x4 trread(LAS char* p) { return __builtin_bit_cast(s16x4, __builtin_amdgcn_ds_read_tr16_b64_v4i16((LAS v4i16_t*)p)); }
__device__ __forceinline__ bf16x8 cat8(s16x4 a, s16x4 b) { return bf16x8{a[0], a[1], a[2], a[3], b[0], b[1], b[2], b[3]}; }
#define WAITV0() asm volatile("s_waitcnt vmcnt(0)" ::: "memory")

__device__ __forceinline__ float gelu_tanh(float x) {
    const float k0 = 0.7978845608028654f, k1 = 0.044715f;
    const float u = k0 * (x + k1 * x * x * x);
    const float e = __expf(-2.0f * u);
    return x / (1.0f + e);
}

__device__ __forceinline__ f2 geglu2(f2 g, f2 u) {
    const float c0 = -2.0f * 0.7978845608028654f * 1.4426950408889634f, c1 = c0 * 0.044715f;
    const f2 z = g * g;
    const f2 t = g * (z * c1 + c0);
    f2 e; e.x = __builtin_amdgcn_exp2f(t.x); e.y = __builtin_amdgcn_exp2f(t.y);
    const f2 d = e + 1.0f;
    f2 r; r.x = __builtin_amdgcn_rcpf(d.x); r.y = __builtin_amdgcn_rcpf(d.y);
    return (g * r) * u;
}
__device__ __forceinline__ f32x4 geglu4(f32x4 g, f32x4 u) {
    const f2 a = geglu2(f2{g[0], g[1]}, f2{u[0], u[1]}), b = geglu2(f2{g[2], g[3]}, f2{u[2], u[3]});
    return f32x4{a.x, a.y, b.x, b.y};
}

#define BAR() do { asm volatile("s_waitcnt lgkmcnt(0)" ::: "memory"); __builtin_amdgcn_s_barrier(); __builtin_amdgcn_sched_barrier(0); } while (0)
#define WAITV(n) asm volatile("s_waitcnt vmcnt(" #n ")" ::: "memory")
constexpr int G1_AB = 32768, G1_BB = 28672, G1_BOFF = 2 * G1_AB, NT1 = D_ / 64;
__global__ __launch_bounds__(512, 2) void k4_gemm1(const u16* __restrict__ A, const u16* __restrict__ Wg, const u16* __restrict__ Wu, u16* __restrict__ act) {
    __shared__ __attribute__((aligned(16))) char smem[2 * G1_AB + 3 * G1_BB];
    LAS char* lds = (LAS char*)smem;
    const int tid = threadIdx.x, lane = tid & 63, wv = __builtin_amdgcn_readfirstlane(tid >> 6);
    const int bid = blockIdx.x, xcd = bid & 7, loc = bid >> 3;
    const int mt = (xcd & 1) * 4 + (loc & 3), nt = (xcd >> 1) * 8 + (loc >> 2);
    const int row0 = mt * 256, col0 = nt * 112;
    const int grp = wv >> 2, w4 = wv & 3;
    const char* src[8];
    size_t kadv;
    if (grp == 0) {
#pragma unroll
        for (int j = 0; j < 8; ++j) {
            const int pc = w4 + 4 * j, r = pc * 8 + (lane >> 3), cp = lane & 7, cc = cp ^ (r & 7);
            src[j] = (const char*)(A + (size_t)(row0 + r) * D_ + cc * 8);
        }
        kadv = 128;
    } else {
#pragma unroll
        for (int j = 0; j < 8; ++j) {
            int pc = w4 + 4 * j; if (pc >= 28) pc = 0;
            const int ch = pc * 64 + lane, blk = ch >> 3, wi = ch & 7, q = wi >> 1, hf = wi & 1;
            const int kb = blk / 14, pos = blk % 14, nb = pos ^ ((kb >> 1) & 1), k = 4 * kb + q;
            src[j] = nb < 7 ? (const char*)(Wg + (size_t)k * FF + col0 + nb * 16 + hf * 8)
                            : (const char*)(Wu + (size_t)k * FF + col0 + (nb - 7) * 16 + (hf ^ 1) * 8);
        }
        kadv = (size_t)64 * FF * 2;
    }
    auto stageA = [&](int slot) {
        LAS char* b = lds + slot * G1_AB + w4 * 1024;
#pragma unroll
        for (int j = 0; j < 8; ++j) { glds16(src[j], b + j * 4096); src[j] += kadv; }
    };
    auto stageB = [&](int slot) {
        LAS char* b = lds + G1_BOFF + slot * G1_BB + w4 * 1024;
#pragma unroll
        for (int j = 0; j < 7; ++j) { glds16(src[j], b + j * 4096); src[j] += kadv; }
    };
    const int fr = lane & 15, fq = lane >> 4;
    const int aoff0 = (w4 * 64 + fr) * 128 + ((fq ^ (fr & 7)) << 4);
    const int aoff1 = (w4 * 64 + fr) * 128 + (((4 + fq) ^ (fr & 7)) << 4);
    int bo[7];
#pragma unroll
    for (int n = 0; n < 7; ++n) {
        const int j = grp * 7 + n, isup = (fr >> 1) & 1, blk = isup * 7 + (j >> 1);
        bo[n] = G1_BOFF + (2 * fq * 14 + (blk ^ (fq & 1))) * 128 + (fr >> 2) * 32 + (((j & 1) ^ isup) << 4) + (fr & 1) * 8;
    }
    f32x4 acc[4][7];
#pragma unroll
    for (int m = 0; m < 4; ++m)
#pragma unroll
        for (int n = 0; n < 7; ++n) acc[m][n] = f32x4{0.f, 0.f, 0.f, 0.f};
    bf16x8 af[2][4], bfr_[2][7];
    auto lphase = [&](int aslot, int bslot, bool doload, LAS char* ldst, int npieces) {
        LAS char* b = lds + bslot * G1_BB;
        LAS char* a = lds + aslot * G1_AB;
#pragma unroll
        for (int i = 0; i < 22; ++i) {
            const int ks = i / 11, q = i % 11;
            if (q < 7) {
                LAS char* pb = b + bo[q] + ks * (8 * 14 * 128);
                bfr_[ks][q] = cat8(trread(pb), trread(pb + 14 * 128));
            } else {
                af[ks][q - 7] = *(LAS bf16x8*)(a + (ks ? aoff1 : aoff0) + (q - 7) * 2048);
            }
            if (i % 3 == 0 && i / 3 < 8) {
                const int j = i / 3;
                if (doload && j < npieces) { glds16(src[j], ldst + j * 4096); src[j] += kadv; }
            }
        }
    };
    auto mma = [&]() {
        __builtin_amdgcn_s_setprio(1);
#pragma unroll
        for (int ks = 0; ks < 2; ++ks)
#pragma unroll
            for (int n = 0; n < 7; ++n)
#pragma unroll
                for (int m = 0; m < 4; ++m) acc[m][n] = __builtin_amdgcn_mfma_f32_16x16x32_bf16(bfr_[ks][n], af[ks][m], acc[m][n], 0, 0, 0);
        __builtin_amdgcn_s_setprio(0);
    };
    if (grp == 0) { stageA(0); WAITV(0); }
    else { stageB(0); stageB(1); WAITV(7); }
    BAR();
    if (grp == 0) {
        int bs = 0;
        for (int t = 0; t < NT1; ++t) {
            lphase(t & 1, bs, t + 1 < NT1, lds + ((t + 1) & 1) * G1_AB + w4 * 1024, 8);
            BAR();
            mma();
            WAITV(0);
            BAR();
            bs = bs == 2 ? 0 : bs + 1;
        }
    } else {
        BAR();
        int bs = 0;
        for (int t = 0; t < NT1; ++t) {
            const int b2 = bs == 0 ? 2 : bs - 1;
            lphase(t & 1, bs, t + 2 < NT1, lds + G1_BOFF + b2 * G1_BB + w4 * 1024, 7);
            if (t + 2 < NT1) { WAITV(7); } else { WAITV(0); }
            BAR();
            mma();
            if (t + 1 < NT1) BAR();
            bs = bs == 2 ? 0 : bs + 1;
        }
    }
    {
        char* ob = (char*)act + ((size_t)((row0 >> 4) + w4 * 4) * (FF / 8) + (col0 >> 3) + grp * 7) * 256 + fr * 16;
        auto grp4 = [&](f32x4& t0, f32x4& t1, f32x4& t2, f32x4& t3, size_t o0, size_t o1, size_t o2, size_t o3) {
            float gp[4], up[4], gq[4], uq[4];
#pragma unroll
            for (int r = 0; r < 4; ++r) {
                auto x = __builtin_amdgcn_permlane32_swap(__float_as_uint(t0[r]), __float_as_uint(t2[r]), false, false);
                gp[r] = __uint_as_float(x[0]); up[r] = __uint_as_float(x[1]);
                auto y = __builtin_amdgcn_permlane32_swap(__float_as_uint(t1[r]), __float_as_uint(t3[r]), false, false);
                gq[r] = __uint_as_float(y[0]); uq[r] = __uint_as_float(y[1]);
            }
            const f2 pa = geglu2(f2{gp[0], gp[1]}, f2{up[0], up[1]}), pb = geglu2(f2{gp[2], gp[3]}, f2{up[2], up[3]});
            const f2 qa = geglu2(f2{gq[0], gq[1]}, f2{uq[0], uq[1]}), qb = geglu2(f2{gq[2], gq[3]}, f2{uq[2], uq[3]});
            const u32 p0 = pack2(pa.x, pa.y), p1 = pack2(pb.x, pb.y), q0 = pack2(qa.x, qa.y), q1 = pack2(qb.x, qb.y);
            auto s0 = __builtin_amdgcn_permlane16_swap(p0, q0, false, false);
            auto s1 = __builtin_amdgcn_permlane16_swap(p1, q1, false, false);
            const size_t o = fq == 0 ? o0 : fq == 1 ? o1 : fq == 2 ? o2 : o3;
            *(u32x4*)(ob + o) = u32x4{s0[0], s1[0], s0[1], s1[1]};
        };
        constexpr size_t MS = (size_t)(FF / 8) * 256;
#pragma unroll
        for (int m = 0; m < 4; ++m)
            grp4(acc[m][0], acc[m][1], acc[m][2], acc[m][3], m * MS, m * MS + 256, m * MS + 512, m * MS + 768);
#pragma unroll
        for (int n = 4; n < 7; ++n)
            grp4(acc[0][n], acc[1][n], acc[2][n], acc[3][n], n * 256, MS + n * 256, 2 * MS + n * 256, 3 * MS + n * 256);
    }
}

constexpr int G2_U = 32768, NU2 = FF / 2 / 64;
__global__ __launch_bounds__(512, 2) void k5_gemm2(const u16* __restrict__ A, const u16* __restrict__ Wd, const u16* __restrict__ h,
                                                   u16* __restrict__ out, f32x4* __restrict__ part, u32* __restrict__ flags) {
    __shared__ __attribute__((aligned(16))) char smem[4 * G2_U];
    LAS char* lds = (LAS char*)smem;
    const int tid = threadIdx.x, lane = tid & 63, wv = __builtin_amdgcn_readfirstlane(tid >> 6);
    const int bid = blockIdx.x, xcd = bid & 7, loc = bid >> 3;
    const int mt = (xcd >> 1) * 4 + (loc & 3), nt = (xcd & 1) * 4 + ((loc >> 2) & 3), ksp = loc >> 4;
    const int row0 = mt * 128, col0 = nt * 128, kbase = ksp * (FF / 2);
    const int grp = wv >> 2, w4 = wv & 3;
    const char* src[8];
#pragma unroll
    for (int j = 0; j < 4; ++j) {
        const int pc = w4 + 4 * j, c = pc >> 1, r = (pc & 1) * 64 + lane;
        src[j] = (const char*)A + ((size_t)((row0 + r) >> 4) * (FF / 8) + (kbase >> 3) + c) * 256 + (r & 15) * 16;
    }
#pragma unroll
    for (int j = 0; j < 4; ++j) {
        const int pc = w4 + 4 * j, ch = pc * 64 + lane, blk = ch >> 3, wi = ch & 7, q = wi >> 1, hf = wi & 1;
        const int kb = blk >> 3, pos = blk & 7, nb = pos ^ ((kb >> 1) & 1), k = 4 * kb + q;
        src[4 + j] = (const char*)(Wd + (size_t)(kbase + k) * D_ + col0 + nb * 16 + hf * 8);
    }
    auto stageA = [&](int u) {
        LAS char* b = lds + (u & 3) * G2_U + w4 * 1024;
        const size_t ao = (size_t)u * 8 * 256;
#pragma unroll
        for (int j = 0; j < 4; ++j) glds16(src[j] + ao, b + j * 4096);
    };
    auto stageB = [&](int u) {
        LAS char* b = lds + (u & 3) * G2_U + w4 * 1024;
        const size_t bo = (size_t)u * 64 * D_ * 2;
#pragma unroll
        for (int j = 0; j < 4; ++j) glds16(src[4 + j] + bo, b + 16384 + j * 4096);
    };
    const int fr = lane & 15, fq = lane >> 4, wr = w4 >> 1, wc = w4 & 1;
    const int aoff = fq * 2048 + (wr * 64 + fr) * 16;
    const int bl = 16384 + (2 * fq * 8) * 128 + (fr >> 2) * 32 + (fr & 3) * 8 + wc * 4 * 128;
    const int sgn = (fq & 1) * 128;
    const int bo0 = bl + sgn, bo1 = bl - sgn;
    f32x4 acc[4][4];
#pragma unroll
    for (int m = 0; m < 4; ++m)
#pragma unroll
        for (int n = 0; n < 4; ++n) acc[m][n] = f32x4{0.f, 0.f, 0.f, 0.f};
    bf16x8 af[2][4], bfr_[2][4];
    auto lphase = [&](int u, bool doload) {
        LAS char* b = lds + (u & 3) * G2_U;
        LAS char* d = lds + ((u + 3) & 3) * G2_U + w4 * 1024;
        const size_t ao = (size_t)(u + 3) * 8 * 256;
#pragma unroll
        for (int i = 0; i < 16; ++i) {
            const int ks = i >> 3, q = i & 7;
            if (q < 4) {
                LAS char* pb = b + ((q & 1) ? bo1 : bo0) + q * 128 + ks * (8 * 8 * 128);
                bfr_[ks][q] = cat8(trread(pb), trread(pb + 8 * 128));
            } else {
                af[ks][q - 4] = *(LAS bf16x8*)(b + aoff + ks * 8192 + (q - 4) * 256);
            }
            if ((i & 3) == 0 && doload) glds16(src[i >> 2] + ao, d + (i >> 2) * 4096);
        }
    };
    auto mma = [&]() {
        __builtin_amdgcn_s_setprio(1);
#pragma unroll
        for (int ks = 0; ks < 2; ++ks)
#pragma unroll
            for (int n = 0; n < 4; ++n)
#pragma unroll
                for (int m = 0; m < 4; ++m) acc[m][n] = __builtin_amdgcn_mfma_f32_16x16x32_bf16(bfr_[ks][n], af[ks][m], acc[m][n], 0, 0, 0);
        __builtin_amdgcn_s_setprio(0);
    };
    if (grp == 0) { stageB(0); stageA(1); stageB(2); } else { stageA(0); stageB(1); stageA(2); }
    WAITV(8);
    BAR();
    if (grp == 0) {
        for (int u = 0; u < NU2; u += 2) {
            lphase(u, u + 3 < NU2);
            if (u + 3 < NU2) { WAITV(8); } else { WAITV(0); }
            BAR();
            mma();
            if (u + 4 < NU2) { stageB(u + 4); WAITV(8); } else { WAITV(0); }
            BAR();
        }
    } else {
        stageB(3);
        WAITV(8);
        BAR();
        for (int u = 1; u < NU2; u += 2) {
            lphase(u, u + 3 < NU2);
            if (u + 3 < NU2) { WAITV(8); } else { WAITV(0); }
            BAR();
            mma();
            if (u + 4 < NU2) { stageB(u + 4); WAITV(8); } else { WAITV(0); }
            if (u + 1 < NU2) BAR();
        }
    }
    __syncthreads();
    LAS f32x4* ex = (LAS f32x4*)lds + w4 * 16 * 64 + lane;
    if (grp == 1) {
#pragma unroll
        for (int m = 0; m < 4; ++m)
#pragma unroll
            for (int n = 0; n < 4; ++n) ex[(m * 4 + n) * 64] = acc[m][n];
    }
    __syncthreads();
    const int tile = mt * 8 + nt;
    if (grp == 0) {
#pragma unroll
        for (int m = 0; m < 4; ++m)
#pragma unroll
            for (int n = 0; n < 4; ++n) acc[m][n] += ex[(m * 4 + n) * 64];
        if (wr != ksp) {
            __amdgpu_buffer_rsrc_t rs = __builtin_amdgcn_make_buffer_rsrc((void*)part, 0, 0x7fffffff, 0x00020000);
            const int off = (((tile * 2 + ksp) * 2 + wc) * 16 * 64 + lane) * 16;
#pragma unroll
            for (int m = 0; m < 4; ++m)
#pragma unroll
                for (int n = 0; n < 4; ++n)
                    __builtin_amdgcn_raw_buffer_store_b128(__builtin_bit_cast(u32x4, acc[m][n]), rs, off + (m * 4 + n) * 1024, 0, 16);
            WAITV(0);
        }
    }
    __syncthreads();
    GAS u32* fl = (GAS u32*)flags;
    if (tid == 0) __hip_atomic_store(fl + (tile * 2 + ksp) * 16, 1u, __ATOMIC_RELAXED, __HIP_MEMORY_SCOPE_AGENT);
    if (grp == 0 && wr == ksp) {
        u32x2 hv[4][4];
        const size_t ro = (size_t)(row0 + wr * 64 + fr) * D_ + col0 + wc * 64 + fq * 4;
#pragma unroll
        for (int m = 0; m < 4; ++m)
#pragma unroll
            for (int n = 0; n < 4; ++n) hv[m][n] = *(const u32x2*)(h + ro + (size_t)m * 16 * D_ + n * 16);
        unsigned spins = 0;
        while (__builtin_amdgcn_readfirstlane(__hip_atomic_load(fl + (tile * 2 + (ksp ^ 1)) * 16, __ATOMIC_RELAXED, __HIP_MEMORY_SCOPE_AGENT)) == 0u) {
            __builtin_amdgcn_s_sleep(1);
            if (++spins > (1u << 22)) break;
        }
        __amdgpu_buffer_rsrc_t rs = __builtin_amdgcn_make_buffer_rsrc((void*)part, 0, 0x7fffffff, 0x00020000);
        const int toff = (((tile * 2 + (ksp ^ 1)) * 2 + wc) * 16 * 64 + lane) * 16;
        f32x4 o[4][4];
#pragma unroll
        for (int m = 0; m < 4; ++m)
#pragma unroll
            for (int n = 0; n < 4; ++n) o[m][n] = __builtin_bit_cast(f32x4, __builtin_amdgcn_raw_buffer_load_b128(rs, toff + (m * 4 + n) * 1024, 0, 16));
#pragma unroll
        for (int m = 0; m < 4; ++m)
#pragma unroll
            for (int n = 0; n < 4; ++n) {
                const f32x4 sum = acc[m][n] + o[m][n];
                float r[4];
#pragma unroll
                for (int j = 0; j < 4; ++j) {
                    const u32 hw = hv[m][n][j >> 1];
                    r[j] = bf2f((j & 1) ? (hw >> 16) : (hw & 0xffffu)) + bfr(sum[j]);
                }
                *(u32x2*)(out + ro + (size_t)m * 16 * D_ + n * 16) = u32x2{pack2(r[0], r[1]), pack2(r[2], r[3])};
            }
    }
}

extern "C" void kernel_launch(void* const* d_in, const int* in_sizes, int n_in, void* d_out, int out_size, void* d_ws, size_t ws_size,
                              hipStream_t stream) {
    const u16* x = (const u16*)d_in[0];
    const u16* wr = (const u16*)d_in[1];
    const u16* wi = (const u16*)d_in[2];
    const u16* wg = (const u16*)d_in[4];
    const u16* wu = (const u16*)d_in[5];
    const u16* wd = (const u16*)d_in[6];
    u16* out = (u16*)d_out;
    char* ws = (char*)d_ws;
    f2* P = (f2*)(ws);
    u32* Wt = (u32*)(ws + (8u << 20));
    u16* h = (u16*)(ws + (13u << 20));
    u16* ln2 = (u16*)(ws + (17u << 20));
    u16* act = (u16*)(ws + (21u << 20));
    f32x4* part = (f32x4*)(ws + (36u << 20));
    u32* flags = (u32*)(ws + (70u << 20));
    k1_ln_fft<<<256 + 288, 512, 0, stream>>>(x, P, wr, wi, Wt, flags, wg);
    k2_col<<<256, 256, 0, stream>>>(P, Wt, wu);
    k3_ifft_ln<<<256, 512, 0, stream>>>(P, x, h, ln2, wd);
    k4_gemm1<<<256, 512, 0, stream>>>(ln2, wg, wu, act);
    k5_gemm2<<<256, 512, 0, stream>>>(act, wd, h, out, part, flags);
}
```

```cpp
#include <hip/hip_runtime.h>
#include <hip/hip_bf16.h>

typedef unsigned short u16;
typedef unsigned int u32;
typedef float f2 __attribute__((ext_vector_type(2)));
typedef float f32x4 __attribute__((ext_vector_type(4)));
typedef short bf16x8 __attribute__((ext_vector_type(8)));
typedef short s16x4 __attribute__((ext_vector_type(4)));
typedef unsigned u32x4 __attribute__((ext_vector_type(4)));
typedef unsigned u32x2 __attribute__((ext_vector_type(2)));
#define LAS __attribute__((address_space(3)))
#define GAS __attribute__((address_space(1)))

constexpr int S_ = 2048, D_ = 1024, DF = 513, FF = 3584;

__device__ __forceinline__ float bf2f(u32 h) { return __uint_as_float(h << 16); }
__device__ __forceinline__ u32 f2bf(float f) { __bf16 b = (__bf16)f; return (u32)__builtin_bit_cast(u16, b); }
__device__ __forceinline__ float bfr(float f) { return bf2f(f2bf(f)); }
typedef __bf16 bf16x2_t __attribute__((ext_vector_type(2)));
__device__ __forceinline__ u32 pack2(float lo, float hi) { return __builtin_bit_cast(u32, __builtin_convertvector(f2{lo, hi}, bf16x2_t)); }

__device__ __forceinline__ f2 add_ib(f2 a, f2 b) {
    f2 r; asm("v_pk_add_f32 %0, %1, %2 op_sel:[0,1] op_sel_hi:[1,0] neg_lo:[0,1]" : "=v"(r) : "v"(a), "v"(b)); return r; }
__device__ __forceinline__ f2 sub_ib(f2 a, f2 b) {
    f2 r; asm("v_pk_add_f32 %0, %1, %2 op_sel:[0,1] op_sel_hi:[1,0] neg_hi:[0,1]" : "=v"(r) : "v"(a), "v"(b)); return r; }
__device__ __forceinline__ f2 add_cj(f2 a, f2 b) {
    f2 r; asm("v_pk_add_f32 %0, %1, %2 neg_hi:[0,1]" : "=v"(r) : "v"(a), "v"(b)); return r; }
__device__ __forceinline__ f2 sub_cj(f2 a, f2 b) {
    f2 r; asm("v_pk_add_f32 %0, %1, %2 neg_lo:[0,1]" : "=v"(r) : "v"(a), "v"(b)); return r; }
__device__ __forceinline__ f2 cj_sub_ib(f2 a, f2 b) {
    f2 r; asm("v_pk_add_f32 %0, %1, %2 op_sel:[0,1] op_sel_hi:[1,0] neg_hi:[1,0]" : "=v"(r) : "v"(a), "v"(b)); return r; }
__device__ __forceinline__ f2 cmul(f2 a, f2 w) {
    f2 t, r;
    asm("v_pk_mul_f32 %0, %1, %2 op_sel:[0,0] op_sel_hi:[0,1]" : "=v"(t) : "v"(a), "v"(w));
    asm("v_pk_fma_f32 %0, %1, %2, %3 op_sel:[1,1,0] op_sel_hi:[1,0,1] neg_lo:[0,1,0]" : "=v"(r) : "v"(a), "v"(w), "v"(t));
    return r; }
__device__ __forceinline__ f2 cmulc(f2 a, f2 w) {
    f2 t, r;
    asm("v_pk_mul_f32 %0, %1, %2 op_sel:[0,0] op_sel_hi:[0,1] neg_hi:[0,1]" : "=v"(t) : "v"(a), "v"(w));
    asm("v_pk_fma_f32 %0, %1, %2, %3 op_sel:[1,1,0] op_sel_hi:[1,0,1]" : "=v"(r) : "v"(a), "v"(w), "v"(t));
    return r; }
template <int D> __device__ __forceinline__ f2 twm(f2 a, f2 w) { return D > 0 ? cmulc(a, w) : cmul(a, w); }
template <int D> __device__ __forceinline__ f2 add_di(f2 a, f2 b) { return D > 0 ? add_ib(a, b) : sub_ib(a, b); }
template <int D> __device__ __forceinline__ f2 sub_di(f2 a, f2 b) { return D > 0 ? sub_ib(a, b) : add_ib(a, b); }

template <int D> __device__ __forceinline__ void dft4(f2& x0, f2& x1, f2& x2, f2& x3) {
    const f2 t0 = x0 + x2, t1 = x0 - x2, t2 = x1 + x3, d = x1 - x3;
    x0 = t0 + t2; x2 = t0 - t2; x1 = add_di<D>(t1, d); x3 = sub_di<D>(t1, d);
}
template <int D> __device__ __forceinline__ void dft8(f2* x) {
    dft4<D>(x[0], x[2], x[4], x[6]);
    dft4<D>(x[1], x[3], x[5], x[7]);
    const float c = 0.70710678118654752440f;
    const f2 o1 = add_di<D>(x[3], x[3]) * c;
    const f2 o3 = sub_di<D>(x[7], x[7]) * c;
    const f2 e0 = x[0], e1 = x[2], e2 = x[4], e3 = x[6], o0 = x[1], o2 = x[5];
    x[0] = e0 + o0; x[4] = e0 - o0;
    x[1] = e1 + o1; x[5] = e1 - o1;
    x[2] = add_di<D>(e2, o2); x[6] = sub_di<D>(e2, o2);
    x[3] = e3 - o3; x[7] = e3 + o3;
}
template <int D> __device__ __forceinline__ void dft16(f2* x) {
    f2 e[8], o[8];
#pragma unroll
    for (int i = 0; i < 8; ++i) { e[i] = x[2 * i]; o[i] = x[2 * i + 1]; }
    dft8<D>(e); dft8<D>(o);
    const float c1 = 0.92387953251128675613f, s1 = 0.38268343236508977173f, c2 = 0.70710678118654752440f;
    o[1] = twm<D>(o[1], f2{c1, -s1});
    o[2] = add_di<D>(o[2], o[2]) * c2;
    o[3] = twm<D>(o[3], f2{s1, -c1});
    o[5] = twm<D>(o[5], f2{-s1, -c1});
    o[6] = sub_di<D>(o[6], o[6]) * c2;
    o[7] = twm<D>(o[7], f2{-c1, -s1});
#pragma unroll
    for (int i = 0; i < 8; ++i) {
        if (i == 4) { x[4] = add_di<D>(e[4], o[4]); x[12] = sub_di<D>(e[4], o[4]); }
        else if (i == 6) { x[6] = e[6] - o[6]; x[14] = e[6] + o[6]; }
        else { x[i] = e[i] + o[i]; x[i + 8] = e[i] - o[i]; }
    }
}
struct TwTab { float v[4096]; };
constexpr double c_sin(double x) { double t = x, r = x; for (int i = 1; i < 12; ++i) { t *= -x * x / ((2 * i) * (2 * i + 1)); r += t; } return r; }
constexpr double c_cos(double x) { double t = 1, r = 1; for (int i = 1; i < 12; ++i) { t *= -x * x / ((2 * i - 1) * (2 * i)); r += t; } return r; }
constexpr TwTab make_tw() {
    TwTab t{};
    const double step = 6.283185307179586476925286766559 / 2048.0;
    for (int k = 0; k < 2048; ++k) {
        const int q = k >> 9, r = k & 511;
        double c = 0, s = 0;
        if (r <= 256) { c = c_cos(r * step); s = c_sin(r * step); } else { c = c_sin((512 - r) * step); s = c_cos((512 - r) * step); }
        double cc = q == 0 ? c : q == 1 ? -s : q == 2 ? -c : s;
        double ss = q == 0 ? s : q == 1 ? c : q == 2 ? -s : -c;
        t.v[2 * k] = (float)cc; t.v[2 * k + 1] = (float)(-ss);
    }
    return t;
}
__device__ const TwTab TW = make_tw();
__device__ __forceinline__ f2 twl(int k) { return *(const f2*)(TW.v + 2 * k); }
template <int N> __device__ __forceinline__ void twchain(f2* w) {
#pragma unroll
    for (int k = 2; k < N; ++k) w[k] = (k & 1) ? cmul(w[k - 1], w[1]) : cmul(w[k / 2], w[k / 2]);
}
__device__ __forceinline__ float wave_sum(float v) {
#pragma unroll
    for (int o = 32; o >= 1; o >>= 1) v += __shfl_xor(v, o);
    return v;
}
__device__ __forceinline__ void ln_row(f2* x) {
    f2 s2 = (x[0] + x[1]) + (x[2] + x[3]) + ((x[4] + x[5]) + (x[6] + x[7]));
    const float mean = wave_sum(s2.x + s2.y) * (1.0f / 1024.0f);
    f2 q2 = f2{0.f, 0.f};
#pragma unroll
    for (int m = 0; m < 8; ++m) { const f2 d = x[m] - mean; q2 += d * d; }
    const float var = wave_sum(q2.x + q2.y) * (1.0f / 1024.0f);
    const float mb = bfr(mean);
    const float rden = 1.0f / bfr(sqrtf(bfr(bfr(var) + 1e-5f)));
#pragma unroll
    for (int m = 0; m < 8; ++m) {
        const f2 d = x[m] - mb;
        const u32 db = pack2(d.x, d.y);
        const f2 q = f2{bf2f(db & 0xffffu), __uint_as_float(db & 0xffff0000u)} * rden;
        const u32 qb = pack2(q.x, q.y);
        x[m] = f2{bf2f(qb & 0xffffu), __uint_as_float(qb & 0xffff0000u)};
    }
}

__device__ __forceinline__ void glds16(const void* g, LAS void* l) {
    unsigned sv; const unsigned lb = (unsigned)__builtin_amdgcn_readfirstlane((unsigned)(size_t)l);
    asm volatile("s_mov_b32 %0, m0\n\ts_mov_b32 m0, %2\n\ts_nop 0\n\tglobal_load_lds_dwordx4 %1, off\n\ts_mov_b32 m0, %0" : "=&s"(sv) : "v"(g), "s"(lb) : "memory");
}
template <int NTH> __device__ __forceinline__ void prefetch_w(const u16* w, int gtid, LAS char* scr) {
#pragma unroll
    for (int i = 0; i < (458752 + NTH - 1) / NTH; ++i) {
        const int ch = gtid + i * NTH;
        if (458752 % NTH == 0 || __builtin_amdgcn_readfirstlane(ch) < 458752) glds16((const char*)w + (size_t)ch * 16, scr);
    }
}
constexpr int RS = 580;
__global__ __launch_bounds__(512) void k1_ln_fft(const u16* __restrict__ x, f2* __restrict__ P, const u16* __restrict__ wr,
                                                 const u16* __restrict__ wi, u32* __restrict__ Wt, u32* __restrict__ flags, const u16* __restrict__ wpf) {
    __shared__ __attribute__((aligned(16))) f2 sm[8 * RS + 1024];
    const int tid = threadIdx.x, lane = tid & 63, wv = tid >> 6;
    if (blockIdx.x >= 256) {
        const int b = blockIdx.x - 256;
        if (b == 0 && tid < 256) flags[tid * 16] = 0u;
        u32* tile = (u32*)sm;
        const int k0 = (b & 31) * 64, c0 = (b >> 5) * 64;
#pragma unroll
        for (int i = 0; i < 8; ++i) {
            const int r = wv + 8 * i, c = c0 + lane;
            u32 v = 0;
            if (c < DF) v = (u32)wr[(k0 + r) * DF + c] | ((u32)wi[(k0 + r) * DF + c] << 16);
            tile[r * 65 + lane] = v;
        }
        __syncthreads();
#pragma unroll
        for (int i = 0; i < 8; ++i) {
            const int c = wv + 8 * i;
            if (c0 + c < DF) Wt[(c0 + c) * S_ + k0 + lane] = tile[lane * 65 + c];
        }
        return;
    }
    const int s = blockIdx.x * 8 + wv;
    const u32* xr = (const u32*)x + (size_t)s * 512;
    f2 v[8], w1[8], w2[8];
    u32 raw[8];
#pragma unroll
    for (int m = 0; m < 8; ++m) raw[m] = xr[lane + 64 * m];
    w1[1] = twl(4 * lane); w2[1] = twl(32 * (lane & 7));
#pragma unroll
    for (int m = 0; m < 8; ++m) v[m] = f2{bf2f(raw[m] & 0xffffu), __uint_as_float(raw[m] & 0xffff0000u)};
    ln_row(v);
    prefetch_w<131072>(wpf, blockIdx.x * 512 + tid, (LAS char*)(sm + 8 * RS) + wv * 1024);
    f2* R = sm + wv * RS;
    dft8<-1>(v);
    twchain<8>(w1);
    R[lane] = v[0];
#pragma unroll
    for (int k = 1; k < 8; ++k) R[k * 72 + lane] = cmul(v[k], w1[k]);
    __builtin_amdgcn_wave_barrier();
    {
        const int k1 = lane >> 3, b = lane & 7;
#pragma unroll
        for (int a = 0; a < 8; ++a) v[a] = R[k1 * 72 + 8 * a + b];
        dft8<-1>(v);
        twchain<8>(w2);
        __builtin_amdgcn_wave_barrier();
        R[k1 * 72 + b * 9] = v[0];
#pragma unroll
        for (int k = 1; k < 8; ++k) R[k1 * 72 + b * 9 + k] = cmul(v[k], w2[k]);
    }
    __builtin_amdgcn_wave_barrier();
    {
        const int k1 = lane & 7, k2 = lane >> 3;
#pragma unroll
        for (int b = 0; b < 8; ++b) v[b] = R[k1 * 72 + 9 * b + k2];
        dft8<-1>(v);
    }
    __builtin_amdgcn_wave_barrier();
#pragma unroll
    for (int k = 0; k < 8; ++k) R[lane + 64 * k] = v[k];
    __syncthreads();
    const int s0 = blockIdx.x * 8;
#pragma unroll
    for (int i = 0; i < 4; ++i) {
        const int it = tid + 512 * i, r = it & 7, p = it >> 3;
        const int ca = p, cb = p ? 512 - p : 256;
        f2 a = sm[r * RS + ca], b = sm[r * RS + cb];
        *(f32x4*)(P + ((size_t)p * S_ + s0 + r) * 2) = f32x4{a.x, a.y, b.x, b.y};
    }
    asm volatile("s_waitcnt vmcnt(0)" ::: "memory");
}

constexpr int CS = 2192;
__device__ __forceinline__ f2 unpk(u32 u) { return f2{bf2f(u & 0xffffu), __uint_as_float(u & 0xffff0000u)}; }
__device__ __forceinline__ void pair_gen(f2& za, f2& zb, u32 fa, u32 fb, f2 tw, f2 twi) {
    const f2 E = add_cj(za, zb), Dd = sub_cj(za, zb);
    const f2 wO = cmul(Dd, twi);
    const f2 Xa = E + wO, Xbc = E - wO;
    const f2 Ya = cmul(Xa, unpk(fa)), Ybc = cmulc(Xbc, unpk(fb));
    const f2 E2 = Ya + Ybc, O2 = cmulc(Ya - Ybc, tw);
    za = add_ib(E2, O2);
    zb = cj_sub_ib(E2, O2);
}
__device__ __forceinline__ void pair_zero(f2& za, f2& zb, u32 f0a, u32 f0b, u32 f5a, u32 f5b) {
    const f2 E = add_cj(za, zb), Dd = sub_cj(za, zb);
    const f2 X0 = sub_ib(E, Dd), X5 = add_ib(E, Dd);
    const f2 s0 = (cmul(X0, unpk(f0a)) + cmulc(X0, unpk(f0b))) * 0.5f;
    const f2 s5 = (cmul(X5, unpk(f5a)) + cmulc(X5, unpk(f5b))) * 0.5f;
    const f2 E2 = s0 + s5, O2 = s0 - s5;
    za = add_ib(E2, O2);
    zb = cj_sub_ib(E2, O2);
}
__global__ __launch_bounds__(256) void k2_col(f2* __restrict__ P, const u32* __restrict__ Wt, const u16* __restrict__ wu) {
    __shared__ __attribute__((aligned(16))) f2 sm[2 * CS + 512];
    const int p = blockIdx.x, tid = threadIdx.x;
    const int c = tid & 1, t = tid >> 1, b = t & 7, k1 = t >> 3;
    f2* base = P + (size_t)p * 4096;
    f2 v[16], w1[16], w2[16];
#pragma unroll
    for (int m = 0; m < 16; ++m) v[m] = base[(128 * m + t) * 2 + c];
    w1[1] = twl(t); w2[1] = twl(16 * b);
    const int T = tid, tt = T & 127, cz = T >> 7;
    const int ka = p ? T : tt, kb = p ? ((256 - T) & 255) : (tt ? 256 - tt : 128);
    const int sa = p ? 0 : cz, sb = p ? 1 : cz;
    const int colA = p ? p : cz * 256, colB = p ? 512 - p : cz * 256;
    const f2 tw = twl(2 * colA);
    u32 wa[8], wb[8], wc[8], wd[8];
#pragma unroll
    for (int j = 0; j < 8; ++j) { wa[j] = Wt[colA * S_ + ka + 256 * j]; wb[j] = Wt[colB * S_ + kb + 256 * j]; }
    if (p == 0) {
#pragma unroll
        for (int j = 0; j < 8; ++j) { wc[j] = Wt[512 * S_ + ka + 256 * j]; wd[j] = Wt[512 * S_ + kb + 256 * j]; }
    }
    twchain<16>(w1); twchain<16>(w2);
    dft16<-1>(v);
    f2* C = sm + c * CS;
    C[t] = v[0];
#pragma unroll
    for (int k = 1; k < 16; ++k) C[k * 136 + t] = cmul(v[k], w1[k]);
    __syncthreads();
    prefetch_w<65536>(wu, p * 256 + tid, (LAS char*)(sm + 2 * CS) + (tid >> 6) * 1024);
#pragma unroll
    for (int a = 0; a < 16; ++a) v[a] = C[k1 * 136 + 8 * a + b];
    dft16<-1>(v);
    __syncthreads();
    C[b * 258 + k1] = v[0];
#pragma unroll
    for (int k = 1; k < 16; ++k) C[b * 258 + k1 + 16 * k] = cmul(v[k], w2[k]);
    __syncthreads();
    f2* A = v; f2* B = v + 8;
    f2* pa = sm + sa * CS + ka; f2* pb = sm + sb * CS + kb;
#pragma unroll
    for (int j = 0; j < 8; ++j) { A[j] = pa[j * 258]; B[j] = pb[j * 258]; }
    dft8<-1>(A); dft8<-1>(B);
    const f2 twi = f2{tw.y, -tw.x};
    if (p) {
        if (T == 0) {
            f2 t0 = B[0]; u32 w0 = wb[0];
#pragma unroll
            for (int j = 0; j < 7; ++j) { B[j] = B[j + 1]; wb[j] = wb[j + 1]; }
            B[7] = t0; wb[7] = w0;
        }
#pragma unroll
        for (int j = 0; j < 8; ++j) pair_gen(A[j], B[7 - j], wa[j], wb[7 - j], tw, twi);
        if (T == 0) {
            f2 t7 = B[7];
#pragma unroll
            for (int j = 7; j > 0; --j) B[j] = B[j - 1];
            B[0] = t7;
        }
    } else if (tt) {
        if (cz) {
#pragma unroll
            for (int j = 0; j < 8; ++j) pair_gen(A[j], B[7 - j], wa[j], wb[7 - j], tw, twi);
        } else {
#pragma unroll
            for (int j = 0; j < 8; ++j) pair_zero(A[j], B[7 - j], wa[j], wb[7 - j], wc[j], wd[7 - j]);
        }
    } else {
        f2 An[8];
#pragma unroll
        for (int j = 0; j < 8; ++j) {
            f2 za = A[j], zb = A[(8 - j) & 7];
            if (cz) pair_gen(za, zb, wa[j], wa[(8 - j) & 7], tw, twi);
            else pair_zero(za, zb, wa[j], wa[(8 - j) & 7], wc[j], wc[(8 - j) & 7]);
            An[j] = za;
        }
#pragma unroll
        for (int j = 0; j < 8; ++j) A[j] = An[j];
#pragma unroll
        for (int j = 0; j < 4; ++j) {
            if (cz) pair_gen(B[j], B[7 - j], wb[j], wb[7 - j], tw, twi);
            else pair_zero(B[j], B[7 - j], wb[j], wb[7 - j], wd[j], wd[7 - j]);
        }
    }
    dft8<1>(A); dft8<1>(B);
    __syncthreads();
#pragma unroll
    for (int j = 0; j < 8; ++j) { pa[j * 258] = A[j]; pb[j * 258] = B[j]; }
    __syncthreads();
    v[0] = C[b * 258 + k1];
#pragma unroll
    for (int k = 1; k < 16; ++k) v[k] = cmulc(C[b * 258 + k1 + 16 * k], w2[k]);
    dft16<1>(v);
    __syncthreads();
#pragma unroll
    for (int a = 0; a < 16; ++a) C[k1 * 136 + 8 * a + b] = v[a];
    __syncthreads();
    v[0] = C[t];
#pragma unroll
    for (int k = 1; k < 16; ++k) v[k] = cmulc(C[k * 136 + t], w1[k]);
    dft16<1>(v);
#pragma unroll
    for (int m = 0; m < 16; ++m) base[(128 * m + t) * 2 + c] = v[m];
    asm volatile("s_waitcnt vmcnt(0)" ::: "memory");
}

__global__ __launch_bounds__(512) void k3_ifft_ln(const f2* __restrict__ P, const u16* __restrict__ x, u16* __restrict__ h, u16* __restrict__ ln2, const u16* __restrict__ wpf) {
    __shared__ __attribute__((aligned(16))) f2 sm[8 * RS + 1024];
    const int tid = threadIdx.x, lane = tid & 63, wv = tid >> 6;
    const int s0 = blockIdx.x * 8, s = s0 + wv;
    const u32* xr = (const u32*)x + (size_t)s * 512;
    f32x4 q[4];
#pragma unroll
    for (int i = 0; i < 4; ++i) {
        const int it = tid + 512 * i, r = it & 7, p = it >> 3;
        q[i] = *(const f32x4*)(P + ((size_t)p * S_ + s0 + r) * 2);
    }
    u32 xraw[8];
#pragma unroll
    for (int m = 0; m < 8; ++m) xraw[m] = xr[lane + 64 * m];
    f2 v[8], w1[8], w2[8];
    w1[1] = twl(4 * lane); w2[1] = twl(32 * (lane & 7));
    twchain<8>(w1); twchain<8>(w2);
#pragma unroll
    for (int i = 0; i < 4; ++i) {
        const int it = tid + 512 * i, r = it & 7, p = it >> 3;
        const int ca = p, cb = p ? 512 - p : 256;
        sm[r * RS + ca] = f2{q[i][0], q[i][1]};
        sm[r * RS + cb] = f2{q[i][2], q[i][3]};
    }
    __syncthreads();
    prefetch_w<131072>(wpf, blockIdx.x * 512 + tid, (LAS char*)(sm + 8 * RS) + wv * 1024);
    f2* R = sm + wv * RS;
#pragma unroll
    for (int k = 0; k < 8; ++k) v[k] = R[lane + 64 * k];
    dft8<1>(v);
    __builtin_amdgcn_wave_barrier();
    {
        const int k1 = lane & 7, k2 = lane >> 3;
#pragma unroll
        for (int b = 0; b < 8; ++b) R[k1 * 72 + 9 * b + k2] = v[b];
    }
    __builtin_amdgcn_wave_barrier();
    {
        const int k1 = lane >> 3, b = lane & 7;
        v[0] = R[k1 * 72 + b * 9];
#pragma unroll
        for (int k = 1; k < 8; ++k) v[k] = cmulc(R[k1 * 72 + b * 9 + k], w2[k]);
        dft8<1>(v);
        __builtin_amdgcn_wave_barrier();
#pragma unroll
        for (int a = 0; a < 8; ++a) R[k1 * 72 + 8 * a + b] = v[a];
    }
    __builtin_amdgcn_wave_barrier();
    v[0] = R[lane];
#pragma unroll
    for (int k = 1; k < 8; ++k) v[k] = cmulc(R[k * 72 + lane], w1[k]);
    dft8<1>(v);
    u32* hr = (u32*)h + (size_t)s * 512;
    u32* lr = (u32*)ln2 + (size_t)s * 512;
    const float NRM = 1.0f / 4194304.0f;
#pragma unroll
    for (int m = 0; m < 8; ++m) {
        const f2 y = v[m] * NRM;
        const f2 hs = unpk(xraw[m]) + unpk(pack2(y.x, y.y));
        const u32 hb = pack2(hs.x, hs.y);
        hr[lane + 64 * m] = hb;
        v[m] = unpk(hb);
    }
    ln_row(v);
#pragma unroll
    for (int m = 0; m < 8; ++m) lr[lane + 64 * m] = pack2(v[m].x, v[m].y);
    asm volatile("s_waitcnt vmcnt(0)" ::: "memory");
}

typedef short v4i16_t __attribute__((ext_vector_type(4)));
__device__ __forceinline__ s16x4 trread(LAS char* p) { return __builtin_bit_cast(s16x4, __builtin_amdgcn_ds_read_tr16_b64_v4i16((LAS v4i16_t*)p)); }
__device__ __forceinline__ bf16x8 cat8(s16x4 a, s16x4 b) { return bf16x8{a[0], a[1], a[2], a[3], b[0], b[1], b[2], b[3]}; }

__device__ __forceinline__ f2 geglu2(f2 g, f2 u) {
    const float c0 = -2.0f * 0.7978845608028654f * 1.4426950408889634f, c1 = c0 * 0.044715f;
    const f2 z = g * g;
    const f2 t = g * (z * c1 + c0);
    f2 e; e.x = __builtin_amdgcn_exp2f(t.x); e.y = __builtin_amdgcn_exp2f(t.y);
    const f2 d = e + 1.0f;
    f2 r; r.x = __builtin_amdgcn_rcpf(d.x); r.y = __builtin_amdgcn_rcpf(d.y);
    return (g * r) * u;
}

#define BAR() do { asm volatile("s_waitcnt lgkmcnt(0)" ::: "memory"); __builtin_amdgcn_s_barrier(); __builtin_amdgcn_sched_barrier(0); } while (0)
#define WAITV(n) asm volatile("s_waitcnt vmcnt(" #n ")" ::: "memory")
constexpr int G1_AB = 32768, G1_BB = 28672, G1_BOFF = 2 * G1_AB, NT1 = D_ / 64;
__global__ __launch_bounds__(512, 2) void k4_gemm1(const u16* __restrict__ A, const u16* __restrict__ Wg, const u16* __restrict__ Wu, u16* __restrict__ act) {
    __shared__ __attribute__((aligned(16))) char smem[2 * G1_AB + 3 * G1_BB];
    LAS char* lds = (LAS char*)smem;
    const int tid = threadIdx.x, lane = tid & 63, wv = __builtin_amdgcn_readfirstlane(tid >> 6);
    const int bid = blockIdx.x, xcd = bid & 7, loc = bid >> 3;
    const int mt = (xcd & 1) * 4 + (loc & 3), nt = (xcd >> 1) * 8 + (loc >> 2);
    const int row0 = mt * 256, col0 = nt * 112;
    const int grp = wv >> 2, w4 = wv & 3;
    const char* src[8];
    size_t kadv;
    if (grp == 0) {
#pragma unroll
        for (int j = 0; j < 8; ++j) {
            const int pc = w4 + 4 * j, r = pc * 8 + (lane >> 3), cp = lane & 7, cc = cp ^ (r & 7);
            src[j] = (const char*)(A + (size_t)(row0 + r) * D_ + cc * 8);
        }
        kadv = 128;
    } else {
#pragma unroll
        for (int j = 0; j < 8; ++j) {
            int pc = w4 + 4 * j; if (pc >= 28) pc = 0;
            const int ch = pc * 64 + lane, blk = ch >> 3, wi = ch & 7, q = wi >> 1, hf = wi & 1;
            const int kb = blk / 14, pos = blk % 14, nb = pos ^ ((kb >> 1) & 1), k = 4 * kb + q;
            src[j] = nb < 7 ? (const char*)(Wg + (size_t)k * FF + col0 + nb * 16 + hf * 8)
                            : (const char*)(Wu + (size_t)k * FF + col0 + (nb - 7) * 16 + (hf ^ 1) * 8);
        }
        kadv = (size_t)64 * FF * 2;
    }
    auto stageA = [&](int slot) {
        LAS char* b = lds + slot * G1_AB + w4 * 1024;
#pragma unroll
        for (int j = 0; j < 8; ++j) { glds16(src[j], b + j * 4096); src[j] += kadv; }
    };
    auto stageB = [&](int slot) {
        LAS char* b = lds + G1_BOFF + slot * G1_BB + w4 * 1024;
#pragma unroll
        for (int j = 0; j < 7; ++j) { glds16(src[j], b + j * 4096); src[j] += kadv; }
    };
    const int fr = lane & 15, fq = lane >> 4;
    const int aoff0 = (w4 * 64 + fr) * 128 + ((fq ^ (fr & 7)) << 4);
    const int aoff1 = (w4 * 64 + fr) * 128 + (((4 + fq) ^ (fr & 7)) << 4);
    int bo[7];
#pragma unroll
    for (int n = 0; n < 7; ++n) {
        const int j = grp * 7 + n, isup = (fr >> 1) & 1, blk = isup * 7 + (j >> 1);
        bo[n] = G1_BOFF + (2 * fq * 14 + (blk ^ (fq & 1))) * 128 + (fr >> 2) * 32 + (((j & 1) ^ isup) << 4) + (fr & 1) * 8;
    }
    f32x4 acc[4][7];
#pragma unroll
    for (int m = 0; m < 4; ++m)
#pragma unroll
        for (int n = 0; n < 7; ++n) acc[m][n] = f32x4{0.f, 0.f, 0.f, 0.f};
    bf16x8 af[2][4], bfr_[2][7];
    auto lphase = [&](int aslot, int bslot, bool doload, LAS char* ldst, int npieces) {
        LAS char* b = lds + bslot * G1_BB;
        LAS char* a = lds + aslot * G1_AB;
#pragma unroll
        for (int i = 0; i < 22; ++i) {
            const int ks = i / 11, q = i % 11;
            if (q < 7) {
                LAS char* pb = b + bo[q] + ks * (8 * 14 * 128);
                bfr_[ks][q] = cat8(trread(pb), trread(pb + 14 * 128));
            } else {
                af[ks][q - 7] = *(LAS bf16x8*)(a + (ks ? aoff1 : aoff0) + (q - 7) * 2048);
            }
            if (i % 3 == 0 && i / 3 < 8) {
                const int j = i / 3;
                if (doload && j < npieces) { glds16(src[j], ldst + j * 4096); src[j] += kadv; }
            }
        }
    };
    auto mma = [&]() {
        __builtin_amdgcn_s_setprio(1);
#pragma unroll
        for (int ks = 0; ks < 2; ++ks)
#pragma unroll
            for (int n = 0; n < 7; ++n)
#pragma unroll
                for (int m = 0; m < 4; ++m) acc[m][n] = __builtin_amdgcn_mfma_f32_16x16x32_bf16(bfr_[ks][n], af[ks][m], acc[m][n], 0, 0, 0);
        __builtin_amdgcn_s_setprio(0);
    };
    if (grp == 0) { stageA(0); WAITV(0); }
    else { stageB(0); stageB(1); WAITV(7); }
    BAR();
    if (grp == 0) {
        int bs = 0;
        for (int t = 0; t < NT1; ++t) {
            lphase(t & 1, bs, t + 1 < NT1, lds + ((t + 1) & 1) * G1_AB + w4 * 1024, 8);
            BAR();
            mma();
            WAITV(0);
            BAR();
            bs = bs == 2 ? 0 : bs + 1;
        }
    } else {
        BAR();
        int bs = 0;
        for (int t = 0; t < NT1; ++t) {
            const int b2 = bs == 0 ? 2 : bs - 1;
            lphase(t & 1, bs, t + 2 < NT1, lds + G1_BOFF + b2 * G1_BB + w4 * 1024, 7);
            if (t + 2 < NT1) { WAITV(7); } else { WAITV(0); }
            BAR();
            mma();
            if (t + 1 < NT1) BAR();
            bs = bs == 2 ? 0 : bs + 1;
        }
    }
    {
        char* ob = (char*)act + ((size_t)((row0 >> 4) + w4 * 4) * (FF / 8) + (col0 >> 3) + grp * 7) * 256 + fr * 16;
        auto grp4 = [&](f32x4& t0, f32x4& t1, f32x4& t2, f32x4& t3, size_t o0, size_t o1, size_t o2, size_t o3) {
            float gp[4], up[4], gq[4], uq[4];
#pragma unroll
            for (int r = 0; r < 4; ++r) {
                auto x = __builtin_amdgcn_permlane32_swap(__float_as_uint(t0[r]), __float_as_uint(t2[r]), false, false);
                gp[r] = __uint_as_float(x[0]); up[r] = __uint_as_float(x[1]);
                auto y = __builtin_amdgcn_permlane32_swap(__float_as_uint(t1[r]), __float_as_uint(t3[r]), false, false);
                gq[r] = __uint_as_float(y[0]); uq[r] = __uint_as_float(y[1]);
            }
            const f2 pa = geglu2(f2{gp[0], gp[1]}, f2{up[0], up[1]}), pb = geglu2(f2{gp[2], gp[3]}, f2{up[2], up[3]});
            const f2 qa = geglu2(f2{gq[0], gq[1]}, f2{uq[0], uq[1]}), qb = geglu2(f2{gq[2], gq[3]}, f2{uq[2], uq[3]});
            const u32 p0 = pack2(pa.x, pa.y), p1 = pack2(pb.x, pb.y), q0 = pack2(qa.x, qa.y), q1 = pack2(qb.x, qb.y);
            auto s0 = __builtin_amdgcn_permlane16_swap(p0, q0, false, false);
            auto s1 = __builtin_amdgcn_permlane16_swap(p1, q1, false, false);
            const size_t o = fq == 0 ? o0 : fq == 1 ? o1 : fq == 2 ? o2 : o3;
            *(u32x4*)(ob + o) = u32x4{s0[0], s1[0], s0[1], s1[1]};
        };
        constexpr size_t MS = (size_t)(FF / 8) * 256;
#pragma unroll
        for (int m = 0; m < 4; ++m)
            grp4(acc[m][0], acc[m][1], acc[m][2], acc[m][3], m * MS, m * MS + 256, m * MS + 512, m * MS + 768);
#pragma unroll
        for (int n = 4; n < 7; ++n)
            grp4(acc[0][n], acc[1][n], acc[2][n], acc[3][n], n * 256, MS + n * 256, 2 * MS + n * 256, 3 * MS + n * 256);
    }
}

constexpr int G2_U = 32768, NU2 = FF / 2 / 64;
__global__ __launch_bounds__(512, 2) void k5_gemm2(const u16* __restrict__ A, const u16* __restrict__ Wd, const u16* __restrict__ h,
                                                   u16* __restrict__ out, f32x4* __restrict__ part, u32* __restrict__ flags) {
    __shared__ __attribute__((aligned(16))) char smem[4 * G2_U];
    LAS char* lds = (LAS char*)smem;
    const int tid = threadIdx.x, lane = tid & 63, wv = __builtin_amdgcn_readfirstlane(tid >> 6);
    const int bid = blockIdx.x, xcd = bid & 7, loc = bid >> 3;
    const int mt = (xcd >> 1) * 4 + (loc & 3), nt = (xcd & 1) * 4 + ((loc >> 2) & 3), ksp = loc >> 4;
    const int row0 = mt * 128, col0 = nt * 128, kbase = ksp * (FF / 2);
    const int grp = wv >> 2, w4 = wv & 3;
    const char* src[8];
#pragma unroll
    for (int j = 0; j < 4; ++j) {
        const int pc = w4 + 4 * j, c = pc >> 1, r = (pc & 1) * 64 + lane;
        src[j] = (const char*)A + ((size_t)((row0 + r) >> 4) * (FF / 8) + (kbase >> 3) + c) * 256 + (r & 15) * 16;
    }
#pragma unroll
    for (int j = 0; j < 4; ++j) {
        const int pc = w4 + 4 * j, ch = pc * 64 + lane, blk = ch >> 3, wi = ch & 7, q = wi >> 1, hf = wi & 1;
        const int kb = blk >> 3, pos = blk & 7, nb = pos ^ ((kb >> 1) & 1), k = 4 * kb + q;
        src[4 + j] = (const char*)(Wd + (size_t)(kbase + k) * D_ + col0 + nb * 16 + hf * 8);
    }
    auto stageA = [&](int u) {
        LAS char* b = lds + (u & 3) * G2_U + w4 * 1024;
        const size_t ao = (size_t)u * 8 * 256;
#pragma unroll
        for (int j = 0; j < 4; ++j) glds16(src[j] + ao, b + j * 4096);
    };
    auto stageB = [&](int u) {
        LAS char* b = lds + (u & 3) * G2_U + w4 * 1024;
        const size_t bo = (size_t)u * 64 * D_ * 2;
#pragma unroll
        for (int j = 0; j < 4; ++j) glds16(src[4 + j] + bo, b + 16384 + j * 4096);
    };
    const int fr = lane & 15, fq = lane >> 4, wr = w4 >> 1, wc = w4 & 1;
    const int aoff = fq * 2048 + (wr * 64 + fr) * 16;
    const int bl = 16384 + (2 * fq * 8) * 128 + (fr >> 2) * 32 + (fr & 3) * 8 + wc * 4 * 128;
    const int sgn = (fq & 1) * 128;
    const int bo0 = bl + sgn, bo1 = bl - sgn;
    f32x4 acc[4][4];
#pragma unroll
    for (int m = 0; m < 4; ++m)
#pragma unroll
        for (int n = 0; n < 4; ++n) acc[m][n] = f32x4{0.f, 0.f, 0.f, 0.f};
    bf16x8 af[2][4], bfr_[2][4];
    auto lphase = [&](int u, bool doload) {
        LAS char* b = lds + (u & 3) * G2_U;
        LAS char* d = lds + ((u + 3) & 3) * G2_U + w4 * 1024;
        const size_t ao = (size_t)(u + 3) * 8 * 256;
#pragma unroll
        for (int i = 0; i < 16; ++i) {
            const int ks = i >> 3, q = i & 7;
            if (q < 4) {
                LAS char* pb = b + ((q & 1) ? bo1 : bo0) + q * 128 + ks * (8 * 8 * 128);
                bfr_[ks][q] = cat8(trread(pb), trread(pb + 8 * 128));
            } else {
                af[ks][q - 4] = *(LAS bf16x8*)(b + aoff + ks * 8192 + (q - 4) * 256);
            }
            if ((i & 3) == 0 && doload) glds16(src[i >> 2] + ao, d + (i >> 2) * 4096);
        }
    };
    auto mma = [&]() {
        __builtin_amdgcn_s_setprio(1);
#pragma unroll
        for (int ks = 0; ks < 2; ++ks)
#pragma unroll
            for (int n = 0; n < 4; ++n)
#pragma unroll
                for (int m = 0; m < 4; ++m) acc[m][n] = __builtin_amdgcn_mfma_f32_16x16x32_bf16(bfr_[ks][n], af[ks][m], acc[m][n], 0, 0, 0);
        __builtin_amdgcn_s_setprio(0);
    };
    if (grp == 0) { stageB(0); stageA(1); stageB(2); } else { stageA(0); stageB(1); stageA(2); }
    WAITV(8);
    BAR();
    if (grp == 0) {
        for (int u = 0; u < NU2; u += 2) {
            lphase(u, u + 3 < NU2);
            if (u + 3 < NU2) { WAITV(8); } else { WAITV(0); }
            BAR();
            mma();
            if (u + 4 < NU2) { stageB(u + 4); WAITV(8); } else { WAITV(0); }
            BAR();
        }
    } else {
        stageB(3);
        WAITV(8);
        BAR();
        for (int u = 1; u < NU2; u += 2) {
            lphase(u, u + 3 < NU2);
            if (u + 3 < NU2) { WAITV(8); } else { WAITV(0); }
            BAR();
            mma();
            if (u + 4 < NU2) { stageB(u + 4); WAITV(8); } else { WAITV(0); }
            if (u + 1 < NU2) BAR();
        }
    }
    __syncthreads();
    LAS f32x4* ex = (LAS f32x4*)lds + w4 * 16 * 64 + lane;
    if (grp == 1) {
#pragma unroll
        for (int m = 0; m < 4; ++m)
#pragma unroll
            for (int n = 0; n < 4; ++n) ex[(m * 4 + n) * 64] = acc[m][n];
    }
    __syncthreads();
    const int tile = mt * 8 + nt;
    if (grp == 0) {
#pragma unroll
        for (int m = 0; m < 4; ++m)
#pragma unroll
            for (int n = 0; n < 4; ++n) acc[m][n] += ex[(m * 4 + n) * 64];
        if (wr != ksp) {
            __amdgpu_buffer_rsrc_t rs = __builtin_amdgcn_make_buffer_rsrc((void*)part, 0, 0x7fffffff, 0x00020000);
            const int off = (((tile * 2 + ksp) * 2 + wc) * 16 * 64 + lane) * 16;
#pragma unroll
            for (int m = 0; m < 4; ++m)
#pragma unroll
                for (int n = 0; n < 4; ++n)
                    __builtin_amdgcn_raw_buffer_store_b128(__builtin_bit_cast(u32x4, acc[m][n]), rs, off + (m * 4 + n) * 1024, 0, 16);
            WAITV(0);
        }
    }
    __syncthreads();
    GAS u32* fl = (GAS u32*)flags;
    if (tid == 0) __hip_atomic_store(fl + (tile * 2 + ksp) * 16, 1u, __ATOMIC_RELAXED, __HIP_MEMORY_SCOPE_AGENT);
    if (grp == 0 && wr == ksp) {
        u32x2 hv[4][4];
        const size_t ro = (size_t)(row0 + wr * 64 + fr) * D_ + col0 + wc * 64 + fq * 4;
#pragma unroll
        for (int m = 0; m < 4; ++m)
#pragma unroll
            for (int n = 0; n < 4; ++n) hv[m][n] = *(const u32x2*)(h + ro + (size_t)m * 16 * D_ + n * 16);
        unsigned spins = 0;
        while (__builtin_amdgcn_readfirstlane(__hip_atomic_load(fl + (tile * 2 + (ksp ^ 1)) * 16, __ATOMIC_RELAXED, __HIP_MEMORY_SCOPE_AGENT)) == 0u) {
            __builtin_amdgcn_s_sleep(1);
            if (++spins > (1u << 22)) break;
        }
        __amdgpu_buffer_rsrc_t rs = __builtin_amdgcn_make_buffer_rsrc((void*)part, 0, 0x7fffffff, 0x00020000);
        const int toff = (((tile * 2 + (ksp ^ 1)) * 2 + wc) * 16 * 64 + lane) * 16;
        f32x4 o[4][4];
#pragma unroll
        for (int m = 0; m < 4; ++m)
#pragma unroll
            for (int n = 0; n < 4; ++n) o[m][n] = __builtin_bit_cast(f32x4, __builtin_amdgcn_raw_buffer_load_b128(rs, toff + (m * 4 + n) * 1024, 0, 16));
#pragma unroll
        for (int m = 0; m < 4; ++m)
#pragma unroll
            for (int n = 0; n < 4; ++n) {
                const f32x4 sum = acc[m][n] + o[m][n];
                float r[4];
#pragma unroll
                for (int j = 0; j < 4; ++j) {
                    const u32 hw = hv[m][n][j >> 1];
                    r[j] = bf2f((j & 1) ? (hw >> 16) : (hw & 0xffffu)) + bfr(sum[j]);
                }
                *(u32x2*)(out + ro + (size_t)m * 16 * D_ + n * 16) = u32x2{pack2(r[0], r[1]), pack2(r[2], r[3])};
            }
    }
}

extern "C" void kernel_launch(void* const* d_in, const int* in_sizes, int n_in, void* d_out, int out_size, void* d_ws, size_t ws_size,
                              hipStream_t stream) {
    const u16* x = (const u16*)d_in[0];
    const u16* wr = (const u16*)d_in[1];
    const u16* wi = (const u16*)d_in[2];
    const u16* wg = (const u16*)d_in[4];
    const u16* wu = (const u16*)d_in[5];
    const u16* wd = (const u16*)d_in[6];
    u16* out = (u16*)d_out;
    char* ws = (char*)d_ws;
    f2* P = (f2*)(ws);
    u32* Wt = (u32*)(ws + (8u << 20));
    u16* h = (u16*)(ws + (13u << 20));
    u16* ln2 = (u16*)(ws + (17u << 20));
    u16* act = (u16*)(ws + (21u << 20));
    f32x4* part = (f32x4*)(ws + (36u << 20));
    u32* flags = (u32*)(ws + (70u << 20));
    k1_ln_fft<<<256 + 288, 512, 0, stream>>>(x, P, wr, wi, Wt, flags, wg);
    k2_col<<<256, 256, 0, stream>>>(P, Wt, wu);
    k3_ifft_ln<<<256, 512, 0, stream>>>(P, x, h, ln2, wd);
    k4_gemm1<<<256, 512, 0, stream>>>(ln2, wg, wu, act);
    k5_gemm2<<<256, 512, 0, stream>>>(act, wd, h, out, part, flags);
}
```
